```python
import torch

def _softmax(x, axis=-1):
    return torch.softmax(x, dim=axis)
def _gelu(x):
    return torch.nn.functional.gelu(x, approximate="tanh")
def _silu(x):
    return torch.nn.functional.silu(x)
def _sigmoid(x):
    return torch.sigmoid(x)
def _layernorm(x, axis=-1, eps=1e-5):
    m = x.mean(dim=axis, keepdim=True)
    v = x.var(dim=axis, keepdim=True, unbiased=False)
    return (x - m) / torch.sqrt(v + eps)
def _scatter_add(tgt, idx, src, axis=0):
    return tgt.clone().index_add_(axis, idx, src)
def _segment_sum(src, seg, n):
    out = torch.zeros((n,) + src.shape[1:], dtype=src.dtype)
    return out.index_add_(0, seg, src)
def _pad(x, pw):
    flat = tuple(v for pair in reversed(pw) for v in pair)
    return torch.nn.functional.pad(x, flat)

kernel_name = 'interleave_layers__linear_kernel_hedgehog__mixer_s_mlp__prompt'

def setup_inputs():

    _w = (lambda a: (lambda t: t.to(torch.bfloat16) if t.is_floating_point() else t)(torch.as_tensor(a)))
    def _A_setup():
        B, S, D, H = 1, 2048, 512, 32
        Dh = D // H
        return {
            "x": _w((lambda *s: torch.randn(*s, dtype=torch.bfloat16) * 0.05)(B, S, D)),
            "wqkv": _w((lambda *s: torch.randn(*s, dtype=torch.bfloat16) * 0.2)(D, 3 * D)),
            "w_phi": _w((lambda *s: torch.randn(*s, dtype=torch.bfloat16) * 0.2)(H, Dh, Dh) * (Dh ** -0.5)),
            "wo": _w((lambda *s: torch.randn(*s, dtype=torch.bfloat16) * 0.05)(D, D)),
        }
    _A_in = _A_setup()

    def _B_setup():
        B, S, D, F = 1, 2048, 512, 1376
        return {
            "x":  _w((lambda *s: torch.randn(*s, dtype=torch.bfloat16) * 0.05)(B, S, D)),
            "w1": _w((lambda *s: torch.randn(*s, dtype=torch.bfloat16) * 3.2)(D, D) * (D ** -0.5)),
            "w2": _w((lambda *s: torch.randn(*s, dtype=torch.bfloat16) * 3.2)(D, D) * (D ** -0.5)),
            "w3": _w((lambda *s: torch.randn(*s, dtype=torch.bfloat16) * 3.2)(D, F) * (D ** -0.5)),
            "w4": _w((lambda *s: torch.randn(*s, dtype=torch.bfloat16) * 3.2)(F, D) * (F ** -0.5)),
        }
    _B_in = _B_setup()

    return {
        "A_x": _A_in["x"],
        "A_wqkv": _A_in["wqkv"],
        "A_w_phi": _A_in["w_phi"],
        "A_wo": _A_in["wo"],
        "B_x": _B_in["x"],
        "B_w1": _B_in["w1"],
        "B_w2": _B_in["w2"],
        "B_w3": _B_in["w3"],
        "B_w4": _B_in["w4"],
    }

def reference(A_x, A_wqkv, A_w_phi, A_wo, B_x, B_w1, B_w2, B_w3, B_w4):
    def _A_ref(x, wqkv, w_phi, wo):
        B, S, D = x.shape
        H = 32; Dh = D // H
        qkv = (lambda x, *d: x.permute(*d))((x @ wqkv).reshape(B, S, 3, H, Dh), 2, 0, 3, 1, 4)
        q, k, v = qkv[0], qkv[1], qkv[2]
        pq = torch.einsum("bhsd,hdf->bhsf", q, w_phi)
        pk = torch.einsum("bhsd,hdf->bhsf", k, w_phi)
        phi_q = torch.concatenate([_softmax(pq, axis=-1), _softmax(-pq, axis=-1)], axis=-1)
        phi_k = torch.concatenate([_softmax(pk, axis=-1), _softmax(-pk, axis=-1)], axis=-1)
        kv = torch.einsum("bhsf,bhsd->bhfd", phi_k, v)
        z  = torch.einsum("bhsf->bhf", phi_k)
        num = torch.einsum("bhqf,bhfd->bhqd", phi_q, kv)
        den = torch.einsum("bhqf,bhf->bhq", phi_q, z)[..., None] + 1e-6
        o = num / den
        return (lambda x, *d: x.permute(*d))(o, 0, 2, 1, 3).reshape(B, S, D) @ wo

    def _B_ref(x, w1, w2, w3, w4):
        B, S, D = x.shape
        h = _gelu(_layernorm(x, axis=-1) @ w1)
        c = D // 4
        g0 = _pad(h[:, :, 0*c:1*c], ((0, 0), (1, 0), (0, 0)))[:, :S, :]
        g1 = _pad(h[:, :, 1*c:2*c], ((0, 0), (0, 1), (0, 0)))[:, 1:, :]
        g2 = _pad(h[:, :, 2*c:3*c], ((0, 0), (2, 0), (0, 0)))[:, :S, :]
        g3 = _pad(h[:, :, 3*c:   ], ((0, 0), (0, 2), (0, 0)))[:, 2:, :]
        hs = torch.concatenate([g0, g1, g2, g3], axis=-1) @ w2
        x1 = x + hs
        h2 = _gelu(_layernorm(x1, axis=-1) @ w3) @ w4
        return x1 + h2

    x = A_x
    x = x + _A_ref(_layernorm(x, axis=-1), A_wqkv, A_w_phi, A_wo)
    x = x + _B_ref(_layernorm(x, axis=-1), B_w1, B_w2, B_w3, B_w4)
    x = x + _A_ref(_layernorm(x, axis=-1), A_wqkv, A_w_phi, A_wo)
    x = x + _B_ref(_layernorm(x, axis=-1), B_w1, B_w2, B_w3, B_w4)
    return x
```

```cpp
#include <hip/hip_runtime.h>
#include <hip/hip_bf16.h>
#include <stdint.h>

typedef __attribute__((ext_vector_type(8))) __bf16 bf16x8;
typedef __attribute__((ext_vector_type(4))) __bf16 bf16x4;
typedef __attribute__((ext_vector_type(4))) float f32x4;
typedef __attribute__((ext_vector_type(4))) unsigned u32x4;
typedef __attribute__((ext_vector_type(2))) unsigned u32x2;
typedef unsigned short u16;

#define LDSP(T, p) ((__attribute__((address_space(3))) T*)(p))

static constexpr int SEQ = 2048;
static constexpr int DM = 512;
static constexpr int FF = 1376;
static constexpr int KVP = 544;

__device__ __forceinline__ f32x4 mfma16(bf16x8 a, bf16x8 b, f32x4 c) {
  return __builtin_amdgcn_mfma_f32_16x16x32_bf16(a, b, c, 0, 0, 0);
}
__device__ __forceinline__ bf16x8 tr_frag(const char* smem, int off0, int off1) {
  bf16x4 v0 = __builtin_amdgcn_ds_read_tr16_b64_v4bf16(LDSP(bf16x4, smem + off0));
  bf16x4 v1 = __builtin_amdgcn_ds_read_tr16_b64_v4bf16(LDSP(bf16x4, smem + off1));
  return __builtin_shufflevector(v0, v1, 0, 1, 2, 3, 4, 5, 6, 7);
}
__device__ __forceinline__ float bfr(float x) { return (float)(__bf16)x; }
__device__ __forceinline__ bf16x4 cvt4(f32x4 v) {
  bf16x4 r;
  r[0] = (__bf16)v[0]; r[1] = (__bf16)v[1]; r[2] = (__bf16)v[2]; r[3] = (__bf16)v[3];
  return r;
}
__device__ __forceinline__ f32x4 up4(bf16x4 v) {
  f32x4 r;
  r[0] = (float)v[0]; r[1] = (float)v[1]; r[2] = (float)v[2]; r[3] = (float)v[3];
  return r;
}
__device__ __forceinline__ bf16x8 pad8(bf16x4 v) {
  bf16x4 z = {(__bf16)0.f, (__bf16)0.f, (__bf16)0.f, (__bf16)0.f};
  return __builtin_shufflevector(v, z, 0, 1, 2, 3, 4, 5, 6, 7);
}
__device__ __forceinline__ bf16x8 ones8() {
  bf16x8 r;
#pragma unroll
  for (int i = 0; i < 8; ++i) r[i] = (__bf16)1.0f;
  return r;
}
__device__ __forceinline__ float gelu_t(float x) {
  float u = 0.7978845608028654f * (x + 0.044715f * x * x * x);
  float e = __expf(-2.0f * u);
  return x * __builtin_amdgcn_rcpf(1.0f + e);
}
__device__ __forceinline__ float sel4(f32x4 v, int i) {
  float a = (i & 1) ? v[1] : v[0];
  float b = (i & 1) ? v[3] : v[2];
  return (i & 2) ? b : a;
}

template <int MODE>
__global__ __launch_bounds__(256, 1) void gemm_s(const u16* __restrict__ A, const u16* __restrict__ W,
                                                 const u16* __restrict__ R1, const u16* __restrict__ R2,
                                                 u16* __restrict__ out, u16* __restrict__ xn_out) {
  constexpr int KS = (MODE == 3) ? 43 : 16;
  constexpr int LDA = KS * 32;
  constexpr int NSLOT = (KS > 32) ? 32 : KS;
  constexpr int G = 4;
  constexpr int NG = (KS + G - 1) / G;
  __shared__ __attribute__((aligned(16))) char smem[NSLOT * 4096 + 256];

  const int b = blockIdx.x;
  const int xcd = b & 7, idx = b >> 3;
  const int mt = xcd * 4 + (idx >> 3), nt = idx & 7;
  const int tid = threadIdx.x, w = tid >> 6, l = tid & 63, r = l & 15, g = l >> 4;
  const int m0 = mt * 64, n0 = nt * 64;
  const int row = m0 + 16 * w + r;

  bf16x8 a[KS];
  u32x4 bw[KS];
  const u16* wp = W + (tid >> 3) * DM + n0 + (tid & 7) * 8;
  bool ok[4] = {true, true, true, true};
  const u16* ap[4];
  if constexpr (MODE == 2) {
    const int sh[4] = {-1, 1, -2, 2};
#pragma unroll
    for (int i = 0; i < 4; ++i) {
      int rr = row + sh[i];
      ok[i] = (rr >= 0) && (rr < SEQ);
      rr = rr < 0 ? 0 : (rr >= SEQ ? SEQ - 1 : rr);
      ap[i] = A + (size_t)rr * LDA + 8 * g;
    }
  } else {
#pragma unroll
    for (int i = 0; i < 4; ++i) ap[i] = A + (size_t)row * LDA + 8 * g;
  }
#pragma unroll
  for (int s = 0; s < KS; ++s) {
    bw[s] = *(const u32x4*)(wp + (size_t)s * 32 * DM);
    a[s] = *(const bf16x8*)(ap[(MODE == 2) ? (s >> 2) : 0] + 32 * s);
  }
  bf16x4 r1[4], r2[4];
  bf16x8 xr[2];
  if constexpr (MODE == 0 || MODE == 2 || MODE == 3) {
#pragma unroll
    for (int c = 0; c < 4; ++c) r1[c] = *(const bf16x4*)(R1 + (size_t)row * DM + n0 + 16 * c + 4 * g);
  }
  if constexpr (MODE == 3) {
#pragma unroll
    for (int c = 0; c < 4; ++c) r2[c] = *(const bf16x4*)(R2 + (size_t)row * DM + n0 + 16 * c + 4 * g);
  }
  if constexpr (MODE == 1) {
#pragma unroll
    for (int i = 0; i < 2; ++i) xr[i] = *(const bf16x8*)(A + (size_t)row * DM + n0 + 32 * i + 8 * g);
  }

  const int kl = tid >> 3, ch = tid & 7;
  const int fw = ((kl >> 1) & 1) | ((kl >> 2) & 2);
  const int woff = kl * 128 + ((((ch >> 1) ^ fw) << 5) | ((ch & 1) << 4));
  const int q = (l & 15) >> 2, p4 = l & 3;
  const int fr = (q >> 1) | ((g & 1) << 1);
  int rb[4];
#pragma unroll
  for (int c = 0; c < 4; ++c) rb[c] = (8 * g + q) * 128 + ((c ^ fr) << 5) + p4 * 8;
  const int rbw = (8 * g + q) * 128 + ((w ^ fr) << 5) + p4 * 8;

  f32x4 acc[4];
#pragma unroll
  for (int c = 0; c < 4; ++c) acc[c] = (f32x4){0.f, 0.f, 0.f, 0.f};
  f32x4 s_acc = {0.f, 0.f, 0.f, 0.f}, q_acc = {0.f, 0.f, 0.f, 0.f}, c_acc = {0.f, 0.f, 0.f, 0.f};
  const bf16x8 ones = ones8();
  const bf16x8 zero8 = pad8((bf16x4){(__bf16)0.f, (__bf16)0.f, (__bf16)0.f, (__bf16)0.f});

#pragma unroll
  for (int gp = 0; gp < NG; ++gp) {
#pragma unroll
    for (int i = 0; i < G; ++i) {
      const int s = gp * G + i;
      if (s < KS) *(u32x4*)(smem + (s % NSLOT) * 4096 + woff) = bw[s];
    }
    __syncthreads();
#pragma unroll
    for (int i = 0; i < G; ++i) {
      const int s = gp * G + i;
      if (s < KS) {
        const int sb = (s % NSLOT) * 4096;
        bf16x8 av = a[s];
        if constexpr (MODE == 2) av = ok[s >> 2] ? av : zero8;
#pragma unroll
        for (int c = 0; c < 4; ++c) {
          bf16x8 wf = tr_frag(smem, sb + rb[c], sb + 512 + rb[c]);
          acc[c] = mfma16(wf, av, acc[c]);
        }
        if constexpr (MODE == 1) {
          bf16x8 wf = tr_frag(smem, sb + rbw, sb + 512 + rbw);
          c_acc = mfma16(wf, ones, c_acc);
          s_acc = mfma16(ones, av, s_acc);
          q_acc = mfma16(av, av, q_acc);
        }
      }
    }
  }

  if constexpr (MODE == 1) {
    float* csb = (float*)(smem + NSLOT * 4096);
    if (r == 0) *(f32x4*)(csb + 16 * w + 4 * g) = c_acc;
    float sq = (g == (r >> 2)) ? sel4(q_acc, r & 3) : 0.f;
    sq += __shfl_xor(sq, 16);
    sq += __shfl_xor(sq, 32);
    const float mean = s_acc[0] * (1.0f / DM);
    float var = sq * (1.0f / DM) - mean * mean;
    var = var < 0.f ? 0.f : var;
    const float rstd = __builtin_amdgcn_rsqf(var + 1e-5f);
    const float scale = rstd * __builtin_amdgcn_rsqf(var * rstd * rstd + 1e-5f);
#pragma unroll
    for (int i = 0; i < 2; ++i) {
      bf16x8 o;
#pragma unroll
      for (int j = 0; j < 8; ++j) o[j] = (__bf16)(((float)xr[i][j] - mean) * rstd);
      *(bf16x8*)(xn_out + (size_t)row * DM + n0 + 32 * i + 8 * g) = o;
    }
    __syncthreads();
#pragma unroll
    for (int c = 0; c < 4; ++c) {
      f32x4 cs = *(const f32x4*)(csb + 16 * c + 4 * g);
      f32x4 v;
#pragma unroll
      for (int j = 0; j < 4; ++j) v[j] = gelu_t(bfr(scale * (acc[c][j] - mean * cs[j])));
      *(bf16x4*)(out + (size_t)row * DM + n0 + 16 * c + 4 * g) = cvt4(v);
    }
  } else {
#pragma unroll
    for (int c = 0; c < 4; ++c) {
      f32x4 v;
      f32x4 x1 = up4(r1[c]);
#pragma unroll
      for (int j = 0; j < 4; ++j) v[j] = x1[j] + bfr(acc[c][j]);
      if constexpr (MODE == 3) {
        f32x4 x2 = up4(r2[c]);
#pragma unroll
        for (int j = 0; j < 4; ++j) v[j] = x2[j] + bfr(v[j]);
      }
      *(bf16x4*)(out + (size_t)row * DM + n0 + 16 * c + 4 * g) = cvt4(v);
    }
  }
}

template <int MODE>
__global__ __launch_bounds__(256, 1) void gemm_l(const u16* __restrict__ A, const u16* __restrict__ W,
                                                 const u16* __restrict__ wphi, u16* __restrict__ out,
                                                 float* __restrict__ kvp) {
  constexpr int KS = 16;
  constexpr int LDW = (MODE == 0) ? 1536 : FF;
  constexpr int NT = (MODE == 0) ? 16 : 15;
  constexpr int PB = 32768;
  constexpr int OFF_CS = 3 * PB;
  constexpr int OFF_ST = OFF_CS + 512;
  constexpr int OFF_PHI = OFF_ST + 1024;
  constexpr int OFF_RED = OFF_PHI + 8192;
  constexpr int LDS_TOTAL = OFF_RED + 4 * 2 * KVP * 4;
  __shared__ __attribute__((aligned(16))) char smem[LDS_TOTAL];

  const int b = blockIdx.x;
  const int xcd = b & 7, idx = b >> 3;
  const int rbk = xcd * 2 + idx / NT, nt = idx % NT;
  const int tid = threadIdx.x, w = tid >> 6, l = tid & 63, r = l & 15, g = l >> 4;
  const int m0 = rbk * 128;
  int pc[3];
  bool pv[3];
#pragma unroll
  for (int p = 0; p < 3; ++p) {
    if constexpr (MODE == 0) { pc[p] = p * 512 + nt * 32; pv[p] = true; }
    else { pc[p] = nt * 96 + p * 32; pv[p] = pc[p] < FF; if (!pv[p]) pc[p] = 0; }
  }
  const int row0 = m0 + 32 * w + r;

  bf16x8 a[2][KS];
  u32x4 bw[4][3][2];
  {
    const u16* ap = A + (size_t)row0 * DM + 8 * g;
#pragma unroll
    for (int j = 0; j < 4; ++j) {
#pragma unroll
      for (int p = 0; p < 3; ++p)
#pragma unroll
        for (int i = 0; i < 2; ++i) {
          const int e = i * 256 + tid;
          const int k = 128 * j + (e >> 2), c = e & 3;
          bw[j][p][i] = *(const u32x4*)(W + (size_t)k * LDW + pc[p] + c * 8);
        }
#pragma unroll
      for (int i = 0; i < 4; ++i) {
        const int s = 4 * j + i;
        a[0][s] = *(const bf16x8*)(ap + 32 * s);
        a[1][s] = *(const bf16x8*)(ap + 16 * DM + 32 * s);
      }
    }
  }
  bf16x8 wph[2];
  if constexpr (MODE == 0) {
#pragma unroll
    for (int hh = 0; hh < 2; ++hh) {
      const u16* pw = wphi + (size_t)(nt * 2 + hh) * 256;
      bf16x4 t4;
#pragma unroll
      for (int j = 0; j < 4; ++j) t4[j] = __builtin_bit_cast(__bf16, pw[(4 * g + j) * 16 + r]);
      wph[hh] = pad8(t4);
    }
  }

  int woff[2];
#pragma unroll
  for (int i = 0; i < 2; ++i) {
    const int e = i * 256 + tid;
    const int k = e >> 2, c = e & 3;
    woff[i] = k * 64 + ((((c >> 1) ^ ((k >> 3) & 1)) << 5) | ((c & 1) << 4));
  }
  const int q = (l & 15) >> 2, p4 = l & 3;
  int rb[2];
#pragma unroll
  for (int c = 0; c < 2; ++c) rb[c] = (8 * g + q) * 64 + ((c ^ (g & 1)) << 5) + p4 * 8;
  const int cs0 = w, cs1 = 4 + (w & 1);
  const int rbc0 = (cs0 >> 1) * PB + (8 * g + q) * 64 + (((cs0 & 1) ^ (g & 1)) << 5) + p4 * 8;
  const int rbc1 = (cs1 >> 1) * PB + (8 * g + q) * 64 + (((cs1 & 1) ^ (g & 1)) << 5) + p4 * 8;

  f32x4 acc[3][2][2];
#pragma unroll
  for (int p = 0; p < 3; ++p)
#pragma unroll
    for (int c = 0; c < 2; ++c)
#pragma unroll
      for (int t = 0; t < 2; ++t) acc[p][c][t] = (f32x4){0.f, 0.f, 0.f, 0.f};
  f32x4 s_acc[2], q_acc[2], c_acc[2];
#pragma unroll
  for (int t = 0; t < 2; ++t) {
    s_acc[t] = (f32x4){0.f, 0.f, 0.f, 0.f};
    q_acc[t] = (f32x4){0.f, 0.f, 0.f, 0.f};
    c_acc[t] = (f32x4){0.f, 0.f, 0.f, 0.f};
  }
  const bf16x8 ones = ones8();

#pragma unroll
  for (int j = 0; j < 4; ++j) {
#pragma unroll
    for (int p = 0; p < 3; ++p)
#pragma unroll
      for (int i = 0; i < 2; ++i) *(u32x4*)(smem + p * PB + j * 8192 + woff[i]) = bw[j][p][i];
    __syncthreads();
#pragma unroll
    for (int i = 0; i < 4; ++i) {
      const int s = 4 * j + i;
      const int sb = s * 2048;
#pragma unroll
      for (int p = 0; p < 3; ++p)
#pragma unroll
        for (int c = 0; c < 2; ++c) {
          bf16x8 wf = tr_frag(smem, p * PB + sb + rb[c], p * PB + sb + 256 + rb[c]);
#pragma unroll
          for (int t = 0; t < 2; ++t) {
            if (MODE == 0 && p == 2) acc[p][c][t] = mfma16(a[t][s], wf, acc[p][c][t]);
            else acc[p][c][t] = mfma16(wf, a[t][s], acc[p][c][t]);
          }
        }
      {
        bf16x8 wf0 = tr_frag(smem, sb + rbc0, sb + 256 + rbc0);
        c_acc[0] = mfma16(wf0, ones, c_acc[0]);
        bf16x8 wf1 = tr_frag(smem, sb + rbc1, sb + 256 + rbc1);
        c_acc[1] = mfma16(wf1, ones, c_acc[1]);
      }
#pragma unroll
      for (int t = 0; t < 2; ++t) {
        s_acc[t] = mfma16(ones, a[t][s], s_acc[t]);
        q_acc[t] = mfma16(a[t][s], a[t][s], q_acc[t]);
      }
    }
  }

  float* csb = (float*)(smem + OFF_CS);
  float* stm = (float*)(smem + OFF_ST);
  float* sts = stm + 128;
  if (r == 0) {
    *(f32x4*)(csb + 16 * cs0 + 4 * g) = c_acc[0];
    if (w < 2) *(f32x4*)(csb + 16 * cs1 + 4 * g) = c_acc[1];
  }
  float mean[2], scale[2];
#pragma unroll
  for (int t = 0; t < 2; ++t) {
    float sq = (g == (r >> 2)) ? sel4(q_acc[t], r & 3) : 0.f;
    sq += __shfl_xor(sq, 16);
    sq += __shfl_xor(sq, 32);
    mean[t] = s_acc[t][0] * (1.0f / DM);
    float var = sq * (1.0f / DM) - mean[t] * mean[t];
    var = var < 0.f ? 0.f : var;
    scale[t] = __builtin_amdgcn_rsqf(var + 1e-5f);
    if (MODE == 0 && g == 0) {
      stm[32 * w + 16 * t + r] = mean[t];
      sts[32 * w + 16 * t + r] = scale[t];
    }
  }
  __syncthreads();

  if constexpr (MODE == 1) {
#pragma unroll
    for (int p = 0; p < 3; ++p) {
      if (pv[p]) {
#pragma unroll
        for (int c = 0; c < 2; ++c) {
          f32x4 cs = *(const f32x4*)(csb + 32 * p + 16 * c + 4 * g);
#pragma unroll
          for (int t = 0; t < 2; ++t) {
            f32x4 v;
#pragma unroll
            for (int j = 0; j < 4; ++j) v[j] = gelu_t(bfr(scale[t] * (acc[p][c][t][j] - mean[t] * cs[j])));
            *(bf16x4*)(out + (size_t)(row0 + 16 * t) * FF + pc[p] + 16 * c + 4 * g) = cvt4(v);
          }
        }
      }
    }
  } else {
    char* phs = smem + OFF_PHI + w * 2048;
    float* red = (float*)(smem + OFF_RED);
    const f32x4 zf = {0.f, 0.f, 0.f, 0.f};
#pragma unroll
    for (int hh = 0; hh < 2; ++hh) {
      const int head = nt * 2 + hh;
      f32x4 csq = *(const f32x4*)(csb + 16 * hh + 4 * g);
      f32x4 csk = *(const f32x4*)(csb + 32 + 16 * hh + 4 * g);
      const float csv = csb[64 + 16 * hh + r];
      bf16x4 vb[2];
#pragma unroll
      for (int t = 0; t < 2; ++t) {
        f32x4 qv, kv4, vv;
#pragma unroll
        for (int j = 0; j < 4; ++j) {
          qv[j] = scale[t] * (acc[0][hh][t][j] - mean[t] * csq[j]);
          kv4[j] = scale[t] * (acc[1][hh][t][j] - mean[t] * csk[j]);
        }
        f32x4 mv = *(const f32x4*)(stm + 32 * w + 16 * t + 4 * g);
        f32x4 sv = *(const f32x4*)(sts + 32 * w + 16 * t + 4 * g);
#pragma unroll
        for (int j = 0; j < 4; ++j) vv[j] = sv[j] * (acc[2][hh][t][j] - mv[j] * csv);
        vb[t] = cvt4(vv);
        f32x4 pq = mfma16(wph[hh], pad8(cvt4(qv)), zf);
        *(bf16x4*)(out + (size_t)(row0 + 16 * t) * DM + head * 16 + 4 * g) = cvt4(pq);
        f32x4 pk = mfma16(wph[hh], pad8(cvt4(kv4)), zf);
#pragma unroll
        for (int j = 0; j < 4; ++j) pk[j] = bfr(pk[j]);
        float mx = fmaxf(fmaxf(pk[0], pk[1]), fmaxf(pk[2], pk[3]));
        float mn = fminf(fminf(pk[0], pk[1]), fminf(pk[2], pk[3]));
        mx = fmaxf(mx, __shfl_xor(mx, 16));
        mx = fmaxf(mx, __shfl_xor(mx, 32));
        mn = fminf(mn, __shfl_xor(mn, 16));
        mn = fminf(mn, __shfl_xor(mn, 32));
        f32x4 ep, en;
        float sp = 0.f, sn = 0.f;
#pragma unroll
        for (int j = 0; j < 4; ++j) {
          ep[j] = __expf(pk[j] - mx); sp += ep[j];
          en[j] = __expf(mn - pk[j]); sn += en[j];
        }
        sp += __shfl_xor(sp, 16);
        sp += __shfl_xor(sp, 32);
        sn += __shfl_xor(sn, 16);
        sn += __shfl_xor(sn, 32);
        const float ip = __builtin_amdgcn_rcpf(sp), in_ = __builtin_amdgcn_rcpf(sn);
#pragma unroll
        for (int j = 0; j < 4; ++j) { ep[j] *= ip; en[j] *= in_; }
        *(bf16x4*)(phs + (16 * t + r) * 64 + 8 * g) = cvt4(ep);
        *(bf16x4*)(phs + (16 * t + r) * 64 + 32 + 8 * g) = cvt4(en);
      }
      const bf16x8 vB = __builtin_shufflevector(vb[0], vb[1], 0, 1, 2, 3, 4, 5, 6, 7);
      float* rd = red + (w * 2 + hh) * KVP;
#pragma unroll
      for (int fh = 0; fh < 2; ++fh) {
        const int o0 = (4 * g + q) * 64 + fh * 32 + p4 * 8;
        bf16x8 pa = tr_frag(phs, o0, o0 + 1024);
        f32x4 kvv = mfma16(pa, vB, zf);
        f32x4 zz = mfma16(pa, ones, zf);
#pragma unroll
        for (int j = 0; j < 4; ++j) rd[(16 * fh + 4 * g + j) * 16 + r] = kvv[j];
        if (r == 0) *(f32x4*)(rd + 512 + 16 * fh + 4 * g) = zz;
      }
    }
    __syncthreads();
    for (int e = tid; e < 2 * KVP / 4; e += 256) {
      const int hh = e / (KVP / 4), i4 = e % (KVP / 4);
      f32x4 sum = *(const f32x4*)(red + (0 * 2 + hh) * KVP + 4 * i4);
#pragma unroll
      for (int ww = 1; ww < 4; ++ww) sum += *(const f32x4*)(red + (ww * 2 + hh) * KVP + 4 * i4);
      *(f32x4*)(kvp + ((size_t)(nt * 2 + hh) * 16 + rbk) * KVP + 4 * i4) = sum;
    }
  }
}

__global__ __launch_bounds__(256, 1) void attn_apply(const u16* __restrict__ pq, const float* __restrict__ kvp,
                                                     u16* __restrict__ o) {
  __shared__ __attribute__((aligned(16))) __bf16 kvT[16 * 32 + 32];
  const int b = blockIdx.x;
  const int chunk = b & 7, head = b >> 3;
  const int tid = threadIdx.x, w = tid >> 6, l = tid & 63, r = l & 15, g = l >> 4;
  const int row0 = chunk * 256 + w * 64 + r;
  bf16x8 pf[4];
#pragma unroll
  for (int t = 0; t < 4; ++t)
    pf[t] = *(const bf16x8*)(pq + (size_t)(row0 + 16 * t) * DM + head * 16 + 8 * (g & 1));
  if (tid < KVP / 4) {
    const float* p = kvp + (size_t)head * 16 * KVP + 4 * tid;
    f32x4 v[16];
#pragma unroll
    for (int i = 0; i < 16; ++i) v[i] = *(const f32x4*)(p + i * KVP);
    f32x4 s = v[0];
#pragma unroll
    for (int i = 1; i < 16; ++i) s += v[i];
    if (tid < 128) {
      const int f = tid >> 2, d0 = (tid & 3) * 4;
#pragma unroll
      for (int j = 0; j < 4; ++j) kvT[(d0 + j) * 32 + f] = (__bf16)s[j];
    } else {
      const int f0 = (tid - 128) * 4;
#pragma unroll
      for (int j = 0; j < 4; ++j) kvT[512 + f0 + j] = (__bf16)s[j];
    }
  }
  bf16x8 ph[4];
  const float sg = (g < 2) ? 1.0f : -1.0f;
#pragma unroll
  for (int t = 0; t < 4; ++t) {
    float v[8];
#pragma unroll
    for (int j = 0; j < 8; ++j) v[j] = sg * (float)pf[t][j];
    float mx = v[0];
#pragma unroll
    for (int j = 1; j < 8; ++j) mx = fmaxf(mx, v[j]);
    mx = fmaxf(mx, __shfl_xor(mx, 16));
    float s = 0.f;
#pragma unroll
    for (int j = 0; j < 8; ++j) { v[j] = __expf(v[j] - mx); s += v[j]; }
    s += __shfl_xor(s, 16);
    const float is = __builtin_amdgcn_rcpf(s);
#pragma unroll
    for (int j = 0; j < 8; ++j) ph[t][j] = (__bf16)(v[j] * is);
  }
  __syncthreads();
  const bf16x8 kvA = *(const bf16x8*)(kvT + r * 32 + 8 * g);
  const bf16x8 zA = *(const bf16x8*)(kvT + 512 + 8 * g);
  const f32x4 zf = {0.f, 0.f, 0.f, 0.f};
#pragma unroll
  for (int t = 0; t < 4; ++t) {
    f32x4 num = mfma16(kvA, ph[t], zf);
    f32x4 den = mfma16(zA, ph[t], zf);
    const float dn = bfr(bfr(den[0]) + 1e-6f);
    f32x4 v;
#pragma unroll
    for (int j = 0; j < 4; ++j) v[j] = bfr(num[j]) / dn;
    *(bf16x4*)(o + (size_t)(row0 + 16 * t) * DM + head * 16 + 4 * g) = cvt4(v);
  }
}

extern "C" void kernel_launch(void* const* d_in, const int* in_sizes, int n_in, void* d_out, int out_size,
                              void* d_ws, size_t ws_size, hipStream_t stream) {
  const u16* x0 = (const u16*)d_in[0];
  const u16* wqkv = (const u16*)d_in[1];
  const u16* wphi = (const u16*)d_in[2];
  const u16* wo = (const u16*)d_in[3];
  const u16* w1 = (const u16*)d_in[5];
  const u16* w2 = (const u16*)d_in[6];
  const u16* w3 = (const u16*)d_in[7];
  const u16* w4 = (const u16*)d_in[8];
  char* ws = (char*)d_ws;
  const size_t T = (size_t)SEQ * DM * 2;
  u16* pq = (u16*)(ws + 0 * T);
  u16* o = (u16*)(ws + 1 * T);
  u16* x1 = (u16*)(ws + 2 * T);
  u16* xn = (u16*)(ws + 3 * T);
  u16* h = (u16*)(ws + 4 * T);
  u16* y1 = (u16*)(ws + 5 * T);
  u16* x2 = (u16*)(ws + 6 * T);
  u16* h2 = (u16*)(ws + 7 * T);
  float* kvp = (float*)(ws + 10 * T);

  for (int layer = 0; layer < 2; ++layer) {
    const u16* xin = layer == 0 ? x0 : x2;
    u16* xout = layer == 0 ? x2 : (u16*)d_out;
    gemm_l<0><<<256, 256, 0, stream>>>(xin, wqkv, wphi, pq, kvp);
    attn_apply<<<256, 256, 0, stream>>>(pq, kvp, o);
    gemm_s<0><<<256, 256, 0, stream>>>(o, wo, xin, nullptr, x1, nullptr);
    gemm_s<1><<<256, 256, 0, stream>>>(x1, w1, nullptr, nullptr, h, xn);
    gemm_s<2><<<256, 256, 0, stream>>>(h, w2, xn, nullptr, y1, nullptr);
    gemm_l<1><<<240, 256, 0, stream>>>(y1, w3, nullptr, h2, nullptr);
    gemm_s<3><<<256, 256, 0, stream>>>(h2, w4, y1, x1, xout, nullptr);
  }
}
```

```cpp
#include <hip/hip_runtime.h>
#include <hip/hip_bf16.h>
#include <stdint.h>

typedef __attribute__((ext_vector_type(8))) __bf16 bf16x8;
typedef __attribute__((ext_vector_type(4))) __bf16 bf16x4;
typedef __attribute__((ext_vector_type(2))) __bf16 bf16x2;
typedef __attribute__((ext_vector_type(4))) float f32x4;
typedef __attribute__((ext_vector_type(4))) unsigned u32x4;
typedef __attribute__((ext_vector_type(2))) unsigned u32x2;
typedef unsigned short u16;

#define LDSP(T, p) ((__attribute__((address_space(3))) T*)(p))
#define GLBP(T, p) ((__attribute__((address_space(1))) T*)(p))

static constexpr int SEQ = 2048;
static constexpr int DM = 512;
static constexpr int FF = 1376;
static constexpr int FFP = 1408;
static constexpr int KVP = 544;

__device__ __forceinline__ f32x4 mfma16(bf16x8 a, bf16x8 b, f32x4 c) {
  return __builtin_amdgcn_mfma_f32_16x16x32_bf16(a, b, c, 0, 0, 0);
}
__device__ __forceinline__ bf16x8 tr_frag(const char* smem, int off0, int off1) {
  bf16x4 v0 = __builtin_amdgcn_ds_read_tr16_b64_v4bf16(LDSP(bf16x4, smem + off0));
  bf16x4 v1 = __builtin_amdgcn_ds_read_tr16_b64_v4bf16(LDSP(bf16x4, smem + off1));
  return __builtin_shufflevector(v0, v1, 0, 1, 2, 3, 4, 5, 6, 7);
}
__device__ __forceinline__ void glds16(const u16* base, unsigned voff, unsigned lds) {
  unsigned keep;
  asm volatile("s_mov_b32 %0, m0\n\ts_mov_b32 m0, %3\n\ts_nop 0\n\tglobal_load_lds_dwordx4 %1, %2\n\ts_mov_b32 m0, %0"
               : "=&s"(keep) : "v"(voff), "s"(base), "s"(lds) : "memory");
}
__device__ __forceinline__ unsigned lds_addr(const char* p) { return (unsigned)(uintptr_t)LDSP(const char, p); }
template <int N>
__device__ __forceinline__ void wait_vm() {
  asm volatile("s_waitcnt vmcnt(%0)" ::"n"(N) : "memory");
}
__device__ __forceinline__ void bar() {
  asm volatile("s_waitcnt lgkmcnt(0)" ::: "memory");
  __builtin_amdgcn_s_barrier();
  asm volatile("" ::: "memory");
}
__device__ __forceinline__ float dot8(bf16x8 a, bf16x8 b, float c) {
  c = __builtin_amdgcn_fdot2_f32_bf16(__builtin_shufflevector(a, a, 0, 1), __builtin_shufflevector(b, b, 0, 1), c, false);
  c = __builtin_amdgcn_fdot2_f32_bf16(__builtin_shufflevector(a, a, 2, 3), __builtin_shufflevector(b, b, 2, 3), c, false);
  c = __builtin_amdgcn_fdot2_f32_bf16(__builtin_shufflevector(a, a, 4, 5), __builtin_shufflevector(b, b, 4, 5), c, false);
  c = __builtin_amdgcn_fdot2_f32_bf16(__builtin_shufflevector(a, a, 6, 7), __builtin_shufflevector(b, b, 6, 7), c, false);
  return c;
}
__device__ __forceinline__ float bfr(float x) { return (float)(__bf16)x; }
__device__ __forceinline__ bf16x4 cvt4(f32x4 v) {
  bf16x4 r;
  r[0] = (__bf16)v[0]; r[1] = (__bf16)v[1]; r[2] = (__bf16)v[2]; r[3] = (__bf16)v[3];
  return r;
}
__device__ __forceinline__ f32x4 up4(bf16x4 v) {
  f32x4 r;
  r[0] = (float)v[0]; r[1] = (float)v[1]; r[2] = (float)v[2]; r[3] = (float)v[3];
  return r;
}
__device__ __forceinline__ bf16x8 pad8(bf16x4 v) {
  bf16x4 z = {(__bf16)0.f, (__bf16)0.f, (__bf16)0.f, (__bf16)0.f};
  return __builtin_shufflevector(v, z, 0, 1, 2, 3, 4, 5, 6, 7);
}
__device__ __forceinline__ bf16x8 ones8() {
  bf16x8 r;
#pragma unroll
  for (int i = 0; i < 8; ++i) r[i] = (__bf16)1.0f;
  return r;
}
__device__ __forceinline__ float gelu_t(float x) {
  float u = 0.7978845608028654f * (x + 0.044715f * x * x * x);
  float e = __expf(-2.0f * u);
  return x * __builtin_amdgcn_rcpf(1.0f + e);
}
__device__ __forceinline__ float red_g(float v) {
  v += __shfl_xor(v, 16);
  v += __shfl_xor(v, 32);
  return v;
}

template <int MODE>
__global__ __launch_bounds__(512, 1) void gemm_s(const u16* __restrict__ A, const u16* __restrict__ W,
                                                 const u16* __restrict__ R, u16* __restrict__ out,
                                                 u16* __restrict__ xn_out) {
  constexpr int OFF_W = 65536, OFF_R = 131072, OFF_M = 139264;
  __shared__ __attribute__((aligned(1024))) char smem[OFF_M + 2048];
  const int b = blockIdx.x;
  const int xcd = b & 7, idx = b >> 3;
  const int mt = xcd * 4 + (idx >> 3), nt = idx & 7;
  const int tid = threadIdx.x, w = __builtin_amdgcn_readfirstlane(tid >> 6), l = tid & 63, r = l & 15, g = l >> 4;
  const int m0 = mt * 64, n0 = nt * 64;

  {
    const unsigned lb = lds_addr(smem) + w * 1024;
    const int lr = l >> 3, lc = (l & 7) ^ lr;
    const unsigned avo = (lr * DM + lc * 8) * 2;
    const u16* abase = A + (size_t)(m0 + 8 * w) * DM;
    const int fk = ((lr >> 1) & 1) | ((w & 1) << 1);
    const int wu = ((l & 7) >> 1) ^ fk;
    const unsigned wvo = (lr * DM + wu * 16 + (l & 1) * 8) * 2;
    const u16* wbase = W + (size_t)(8 * w) * DM + n0;
    if constexpr (MODE != 1) glds16(R + (size_t)(m0 + 8 * w) * DM + n0, avo, lb + OFF_R);
#pragma unroll
    for (int c = 0; c < 8; ++c) {
      glds16(wbase + c * 64 * DM, wvo, lb + OFF_W + c * 8192);
      const int sh = (MODE == 2) ? ((c >> 1) == 0 ? -1 : (c >> 1) == 1 ? 1 : (c >> 1) == 2 ? -2 : 2) : 0;
      glds16(abase + c * 64 + sh * DM, avo, lb + c * 8192);
    }
  }

  const int rt = w >> 1, ch = w & 1;
  const int arow = 16 * rt + r;
  int aoff[2];
#pragma unroll
  for (int h = 0; h < 2; ++h) aoff[h] = arow * 128 + (((4 * h + g) ^ (r & 7)) << 4);
  const int q = r >> 2, p4 = l & 3;
  const int fr = (q >> 1) | ((g & 1) << 1);
  int woff[2];
#pragma unroll
  for (int c = 0; c < 2; ++c) woff[c] = OFF_W + (8 * g + q) * 128 + (((2 * ch + c) ^ fr) << 5) + p4 * 8;

  f32x4 acc[2];
  acc[0] = (f32x4){0.f, 0.f, 0.f, 0.f};
  acc[1] = (f32x4){0.f, 0.f, 0.f, 0.f};
  float ssum = 0.f, ssq = 0.f, cs0 = 0.f, cs1 = 0.f;
  const bf16x8 ones = ones8();

#define S_STEP(S)                                                                              \
  {                                                                                            \
    wait_vm<(3 - (S)) * 4>();                                                                  \
    bar();                                                                                     \
    _Pragma("unroll") for (int cc = 0; cc < 2; ++cc) {                                         \
      _Pragma("unroll") for (int h = 0; h < 2; ++h) {                                          \
        const int cb = (2 * (S) + cc) * 8192;                                                  \
        bf16x8 a = *(const bf16x8*)(smem + cb + aoff[h]);                                      \
        bf16x8 wf0 = tr_frag(smem, cb + h * 4096 + woff[0], cb + h * 4096 + 512 + woff[0]);    \
        bf16x8 wf1 = tr_frag(smem, cb + h * 4096 + woff[1], cb + h * 4096 + 512 + woff[1]);    \
        acc[0] = mfma16(wf0, a, acc[0]);                                                       \
        acc[1] = mfma16(wf1, a, acc[1]);                                                       \
        if constexpr (MODE == 1) {                                                             \
          if (h == ch) { ssum = dot8(a, ones, ssum); ssq = dot8(a, a, ssq); }                  \
          if (2 * cc + h == rt) { cs0 = dot8(wf0, ones, cs0); cs1 = dot8(wf1, ones, cs1); }    \
        }                                                                                      \
      }                                                                                        \
    }                                                                                          \
  }
  S_STEP(0) S_STEP(1) S_STEP(2) S_STEP(3)
#undef S_STEP

  const int row = m0 + arow;
  if constexpr (MODE == 1) {
    float* st = (float*)(smem + OFF_M);
    float* csp = (float*)(smem + OFF_M + 1024);
    ssum = red_g(ssum); ssq = red_g(ssq);
    cs0 = red_g(cs0); cs1 = red_g(cs1);
    if (g == 0) {
      *(float2*)(st + (ch * 64 + arow) * 2) = make_float2(ssum, ssq);
      csp[rt * 64 + 32 * ch + r] = cs0;
      csp[rt * 64 + 32 * ch + 16 + r] = cs1;
    }
    bar();
    const float2 s0 = *(const float2*)(st + arow * 2), s1 = *(const float2*)(st + (64 + arow) * 2);
    const float mean = (s0.x + s1.x) * (1.0f / DM);
    float var = (s0.y + s1.y) * (1.0f / DM) - mean * mean;
    var = var < 0.f ? 0.f : var;
    const float rstd = __builtin_amdgcn_rsqf(var + 1e-5f);
    const float scale = rstd * __builtin_amdgcn_rsqf(var * rstd * rstd + 1e-5f);
#pragma unroll
    for (int c = 0; c < 2; ++c) {
      const int u = 2 * ch + c;
      f32x4 cs = *(const f32x4*)(csp + 16 * u + 4 * g);
#pragma unroll
      for (int i = 1; i < 4; ++i) cs += *(const f32x4*)(csp + i * 64 + 16 * u + 4 * g);
      f32x4 v;
#pragma unroll
      for (int j = 0; j < 4; ++j) v[j] = gelu_t(bfr(scale * (acc[c][j] - mean * cs[j])));
      *(bf16x4*)(out + (size_t)row * DM + n0 + 16 * u + 4 * g) = cvt4(v);
      f32x4 x = up4(*(const bf16x4*)(smem + nt * 8192 + arow * 128 + (((2 * u + (g >> 1)) ^ (r & 7)) << 4) + (g & 1) * 8));
#pragma unroll
      for (int j = 0; j < 4; ++j) x[j] = (x[j] - mean) * rstd;
      *(bf16x4*)(xn_out + (size_t)row * DM + n0 + 16 * u + 4 * g) = cvt4(x);
    }
    if ((mt == 0 || mt == 31) && tid < 16) {
      const int pr = (mt == 0) ? -2 + (tid >> 3) : SEQ + (tid >> 3);
      *(u32x4*)(out + (ptrdiff_t)pr * DM + n0 + (tid & 7) * 8) = (u32x4){0u, 0u, 0u, 0u};
    }
  } else {
#pragma unroll
    for (int c = 0; c < 2; ++c) {
      const int u = 2 * ch + c;
      f32x4 x = up4(*(const bf16x4*)(smem + OFF_R + arow * 128 + (((2 * u + (g >> 1)) ^ (r & 7)) << 4) + (g & 1) * 8));
      f32x4 v;
#pragma unroll
      for (int j = 0; j < 4; ++j) v[j] = x[j] + bfr(acc[c][j]);
      *(bf16x4*)(out + (size_t)row * DM + n0 + 16 * u + 4 * g) = cvt4(v);
    }
  }
}

__global__ __launch_bounds__(512, 1) void gemm_b4(const u16* __restrict__ A, const u16* __restrict__ W,
                                                  const u16* __restrict__ R1, const u16* __restrict__ R2,
                                                  u16* __restrict__ out) {
  constexpr int OFF_R = 131072;
  __shared__ __attribute__((aligned(1024))) char smem[OFF_R + 16384];
  const int b = blockIdx.x;
  const int xcd = b & 7, idx = b >> 3;
  const int mt = xcd * 4 + (idx >> 3), nt = idx & 7;
  const int tid = threadIdx.x, w = __builtin_amdgcn_readfirstlane(tid >> 6), l = tid & 63, r = l & 15, g = l >> 4;
  const int m0 = mt * 64, n0 = nt * 64;

  const unsigned lb = lds_addr(smem) + w * 1024;
  const int lr = l >> 3, lc = (l & 7) ^ lr;
  const unsigned avo = (lr * FFP + lc * 8) * 2;
  const unsigned rvo = (lr * DM + lc * 8) * 2;
  const u16* abase = A + (size_t)(m0 + 8 * w) * FFP;
  const int fk = ((lr >> 1) & 1) | ((w & 1) << 1);
  const int wu = ((l & 7) >> 1) ^ fk;
  const unsigned wvo = (lr * DM + wu * 16 + (l & 1) * 8) * 2;
  const u16* wbase = W + (size_t)(8 * w) * DM + n0;
  const u16* wbase_last = W + (size_t)(1344 + 8 * (w & 3)) * DM + n0;
  auto issue = [&](int s) {
    const unsigned sb = lb + (s & 3) * 32768;
    glds16(wbase + (size_t)(2 * s) * 64 * DM, wvo, sb + 16384);
    glds16(abase + (2 * s) * 64, avo, sb);
    if (s == 10) glds16(wbase_last, wvo, sb + 24576);
    else glds16(wbase + (size_t)(2 * s + 1) * 64 * DM, wvo, sb + 24576);
    glds16(abase + (2 * s + 1) * 64, avo, sb + 8192);
  };
  glds16(R1 + (size_t)(m0 + 8 * w) * DM + n0, rvo, lb + OFF_R);
  glds16(R2 + (size_t)(m0 + 8 * w) * DM + n0, rvo, lb + OFF_R + 8192);
  issue(0); issue(1); issue(2); issue(3);

  const int rt = w >> 1, ch = w & 1;
  const int arow = 16 * rt + r;
  int aoff[2];
#pragma unroll
  for (int h = 0; h < 2; ++h) aoff[h] = arow * 128 + (((4 * h + g) ^ (r & 7)) << 4);
  const int q = r >> 2, p4 = l & 3;
  const int fr = (q >> 1) | ((g & 1) << 1);
  int woff[2];
#pragma unroll
  for (int c = 0; c < 2; ++c) woff[c] = 16384 + (8 * g + q) * 128 + (((2 * ch + c) ^ fr) << 5) + p4 * 8;

  f32x4 acc[2];
  acc[0] = (f32x4){0.f, 0.f, 0.f, 0.f};
  acc[1] = (f32x4){0.f, 0.f, 0.f, 0.f};

#define B4_STEP(S)                                                                             \
  {                                                                                            \
    wait_vm<4 * (((S) == 0 ? 3 : ((S) + 2 > 10 ? 10 : (S) + 2)) - (S))>();                    \
    bar();                                                                                     \
    if ((S) >= 1 && (S) + 3 <= 10) issue((S) + 3);                                             \
    _Pragma("unroll") for (int cc = 0; cc < 2; ++cc) {                                         \
      _Pragma("unroll") for (int h = 0; h < 2; ++h) {                                          \
        if ((S) < 10 || cc == 0 || h == 0) {                                                   \
          const int cb = ((S) & 3) * 32768 + cc * 8192;                                        \
          bf16x8 a = *(const bf16x8*)(smem + cb + aoff[h]);                                    \
          bf16x8 wf0 = tr_frag(smem, cb + h * 4096 + woff[0], cb + h * 4096 + 512 + woff[0]);  \
          bf16x8 wf1 = tr_frag(smem, cb + h * 4096 + woff[1], cb + h * 4096 + 512 + woff[1]);  \
          acc[0] = mfma16(wf0, a, acc[0]);                                                     \
          acc[1] = mfma16(wf1, a, acc[1]);                                                     \
        }                                                                                      \
      }                                                                                        \
    }                                                                                          \
  }
  B4_STEP(0) B4_STEP(1) B4_STEP(2) B4_STEP(3) B4_STEP(4) B4_STEP(5)
  B4_STEP(6) B4_STEP(7) B4_STEP(8) B4_STEP(9) B4_STEP(10)
#undef B4_STEP

  const int row = m0 + arow;
#pragma unroll
  for (int c = 0; c < 2; ++c) {
    const int u = 2 * ch + c;
    const int ro = OFF_R + arow * 128 + (((2 * u + (g >> 1)) ^ (r & 7)) << 4) + (g & 1) * 8;
    f32x4 x1 = up4(*(const bf16x4*)(smem + ro));
    f32x4 x2 = up4(*(const bf16x4*)(smem + ro + 8192));
    f32x4 v;
#pragma unroll
    for (int j = 0; j < 4; ++j) v[j] = x2[j] + bfr(x1[j] + bfr(acc[c][j]));
    *(bf16x4*)(out + (size_t)row * DM + n0 + 16 * u + 4 * g) = cvt4(v);
  }
}

template <int MODE>
__global__ __launch_bounds__(512, 1) void gemm_l(const u16* __restrict__ A, const u16* __restrict__ W,
                                                 const u16* __restrict__ wphi, u16* __restrict__ out,
                                                 float* __restrict__ kvp) {
  constexpr int LDW = (MODE == 0) ? 1536 : FF;
  constexpr int NT = (MODE == 0) ? 16 : 15;
  constexpr int OFF_A = 98304;
  constexpr int OFF_ST = 0, OFF_CS = 1024, OFF_PHI = 4096, OFF_RED = 20480;
  __shared__ __attribute__((aligned(1024))) char smem[163840];

  const int b = blockIdx.x;
  const int xcd = b & 7, idx = b >> 3;
  const int rbk = xcd * 2 + idx / NT, nt = idx % NT;
  const int tid = threadIdx.x, w = __builtin_amdgcn_readfirstlane(tid >> 6), l = tid & 63, r = l & 15, g = l >> 4;
  const int m0 = rbk * 128;
  const int rg = w >> 1, hh = w & 1;

  bf16x8 wph;
  if constexpr (MODE == 0) {
    const u16* pw = wphi + (size_t)(nt * 2 + hh) * 256;
    bf16x4 t4;
#pragma unroll
    for (int j = 0; j < 4; ++j) t4[j] = __builtin_bit_cast(__bf16, pw[(4 * g + j) * 16 + r]);
    wph = pad8(t4);
  }

  const unsigned lb = lds_addr(smem);
  const int lr = l >> 3, lc = (l & 7) ^ lr;
  const unsigned avo = (lr * DM + lc * 8) * 2;
  const u16* abase = A + (size_t)(m0 + 16 * w) * DM;
  unsigned wvo[3];
#pragma unroll
  for (int bb = 0; bb < 3; ++bb) {
    const int s16 = 64 * bb + l;
    const int kk = s16 / 12, j = s16 % 12;
    const int lu = (j >> 1) ^ ((kk >> 3) & 1);
    int col;
    if constexpr (MODE == 0) col = (lu >> 1) * 512 + nt * 32 + (lu & 1) * 16 + (j & 1) * 8;
    else { col = nt * 96 + lu * 16 + (j & 1) * 8; if (col >= FF) col -= 64; }
    wvo[bb] = (kk * LDW + col) * 2;
  }
  const u16* wbase = W + (size_t)(16 * w) * LDW;
  auto issueW = [&](int m) {
#pragma unroll
    for (int bb = 0; bb < 3; ++bb) glds16(wbase + (size_t)m * 128 * LDW, wvo[bb], lb + m * 24576 + w * 3072 + bb * 1024);
  };
  auto issueA = [&](int c) {
    glds16(abase + c * 64, avo, lb + OFF_A + (c & 3) * 16384 + w * 2048);
    glds16(abase + 8 * DM + c * 64, avo, lb + OFF_A + (c & 3) * 16384 + w * 2048 + 1024);
  };
  issueW(0); issueA(0); issueA(1); issueW(1); issueA(2); issueA(3); issueW(2); issueW(3);

  int aoff[2];
#pragma unroll
  for (int h = 0; h < 2; ++h) aoff[h] = OFF_A + (32 * rg + r) * 128 + (((4 * h + g) ^ (r & 7)) << 4);
  const int q = r >> 2, p4 = l & 3;
  int woff[3];
#pragma unroll
  for (int p = 0; p < 3; ++p) {
    const int u = (MODE == 0) ? 2 * p + hh : 3 * hh + p;
    woff[p] = (8 * g + q) * 192 + ((u ^ (g & 1)) << 5) + p4 * 8;
  }

  f32x4 acc[3][2];
#pragma unroll
  for (int p = 0; p < 3; ++p)
#pragma unroll
    for (int t = 0; t < 2; ++t) acc[p][t] = (f32x4){0.f, 0.f, 0.f, 0.f};
  float ssum = 0.f, ssq = 0.f, cs[3] = {0.f, 0.f, 0.f};
  const bf16x8 ones = ones8();

#define L_STEP(C, NW)                                                                          \
  {                                                                                            \
    wait_vm<NW>();                                                                             \
    bar();                                                                                     \
    if ((C) >= 1 && (C) + 3 <= 7) issueA((C) + 3);                                             \
    _Pragma("unroll") for (int h = 0; h < 2; ++h) {                                            \
      const int ab = ((C) & 3) * 16384;                                                        \
      const int wb = ((C) * 64 + 32 * h) * 192;                                                \
      bf16x8 a0 = *(const bf16x8*)(smem + ab + aoff[h]);                                       \
      bf16x8 a1 = *(const bf16x8*)(smem + ab + 2048 + aoff[h]);                                \
      bf16x8 wf[3];                                                                            \
      _Pragma("unroll") for (int p = 0; p < 3; ++p) wf[p] = tr_frag(smem, wb + woff[p], wb + 768 + woff[p]); \
      _Pragma("unroll") for (int p = 0; p < 3; ++p) {                                          \
        if (MODE == 0 && p == 2) {                                                             \
          acc[p][0] = mfma16(a0, wf[p], acc[p][0]);                                            \
          acc[p][1] = mfma16(a1, wf[p], acc[p][1]);                                            \
        } else {                                                                               \
          acc[p][0] = mfma16(wf[p], a0, acc[p][0]);                                            \
          acc[p][1] = mfma16(wf[p], a1, acc[p][1]);                                            \
        }                                                                                      \
      }                                                                                        \
      if (hh) { ssum = dot8(a1, ones, ssum); ssq = dot8(a1, a1, ssq); }                        \
      else { ssum = dot8(a0, ones, ssum); ssq = dot8(a0, a0, ssq); }                           \
      if (((2 * (C) + h) & 3) == rg) {                                                         \
        _Pragma("unroll") for (int p = 0; p < 3; ++p) cs[p] = dot8(wf[p], ones, cs[p]);        \
      }                                                                                        \
    }                                                                                          \
  }
  L_STEP(0, 15) L_STEP(1, 13) L_STEP(2, 10) L_STEP(3, 10) L_STEP(4, 4) L_STEP(5, 4) L_STEP(6, 2) L_STEP(7, 0)
#undef L_STEP

  float* stm = (float*)(smem + OFF_ST);
  float* sts = stm + 128;
  float* csp = (float*)(smem + OFF_CS);
  {
    ssum = red_g(ssum); ssq = red_g(ssq);
    const float mean = ssum * (1.0f / DM);
    float var = ssq * (1.0f / DM) - mean * mean;
    var = var < 0.f ? 0.f : var;
    const float rstd = __builtin_amdgcn_rsqf(var + 1e-5f);
#pragma unroll
    for (int p = 0; p < 3; ++p) cs[p] = red_g(cs[p]);
    if (g == 0) {
      stm[32 * rg + 16 * hh + r] = mean;
      sts[32 * rg + 16 * hh + r] = rstd;
#pragma unroll
      for (int p = 0; p < 3; ++p) {
        const int u = (MODE == 0) ? 2 * p + hh : 3 * hh + p;
        csp[rg * 96 + 16 * u + r] = cs[p];
      }
    }
  }
  bar();
  float mean[2], scale[2];
#pragma unroll
  for (int t = 0; t < 2; ++t) {
    mean[t] = stm[32 * rg + 16 * t + r];
    scale[t] = sts[32 * rg + 16 * t + r];
  }
  const int row0 = m0 + 32 * rg + r;

  if constexpr (MODE == 1) {
#pragma unroll
    for (int p = 0; p < 3; ++p) {
      const int u = 3 * hh + p;
      const int col = nt * 96 + 16 * u;
      if (col < FF) {
        f32x4 c4 = *(const f32x4*)(csp + 16 * u + 4 * g);
#pragma unroll
        for (int i = 1; i < 4; ++i) c4 += *(const f32x4*)(csp + i * 96 + 16 * u + 4 * g);
#pragma unroll
        for (int t = 0; t < 2; ++t) {
          f32x4 v;
#pragma unroll
          for (int j = 0; j < 4; ++j) v[j] = gelu_t(bfr(scale[t] * (acc[p][t][j] - mean[t] * c4[j])));
          *(bf16x4*)(out + (size_t)(row0 + 16 * t) * FFP + col + 4 * g) = cvt4(v);
        }
      }
    }
  } else {
    char* phs = smem + OFF_PHI + w * 2048;
    float* red = (float*)(smem + OFF_RED);
    const f32x4 zf = {0.f, 0.f, 0.f, 0.f};
    const int head = nt * 2 + hh;
    f32x4 csq = *(const f32x4*)(csp + 16 * hh + 4 * g);
    f32x4 csk = *(const f32x4*)(csp + 32 + 16 * hh + 4 * g);
    float csv = csp[64 + 16 * hh + r];
#pragma unroll
    for (int i = 1; i < 4; ++i) {
      csq += *(const f32x4*)(csp + i * 96 + 16 * hh + 4 * g);
      csk += *(const f32x4*)(csp + i * 96 + 32 + 16 * hh + 4 * g);
      csv += csp[i * 96 + 64 + 16 * hh + r];
    }
    bf16x4 vb[2];
#pragma unroll
    for (int t = 0; t < 2; ++t) {
      f32x4 qv, kv4, vv;
#pragma unroll
      for (int j = 0; j < 4; ++j) {
        qv[j] = scale[t] * (acc[0][t][j] - mean[t] * csq[j]);
        kv4[j] = scale[t] * (acc[1][t][j] - mean[t] * csk[j]);
      }
      f32x4 mv = *(const f32x4*)(stm + 32 * rg + 16 * t + 4 * g);
      f32x4 sv = *(const f32x4*)(sts + 32 * rg + 16 * t + 4 * g);
#pragma unroll
      for (int j = 0; j < 4; ++j) vv[j] = sv[j] * (acc[2][t][j] - mv[j] * csv);
      vb[t] = cvt4(vv);
      f32x4 pq = mfma16(wph, pad8(cvt4(qv)), zf);
      *(bf16x4*)(out + (size_t)(row0 + 16 * t) * DM + head * 16 + 4 * g) = cvt4(pq);
      f32x4 pk = mfma16(wph, pad8(cvt4(kv4)), zf);
#pragma unroll
      for (int j = 0; j < 4; ++j) pk[j] = bfr(pk[j]);
      float mx = fmaxf(fmaxf(pk[0], pk[1]), fmaxf(pk[2], pk[3]));
      float mn = fminf(fminf(pk[0], pk[1]), fminf(pk[2], pk[3]));
      mx = fmaxf(mx, __shfl_xor(mx, 16));
      mx = fmaxf(mx, __shfl_xor(mx, 32));
      mn = fminf(mn, __shfl_xor(mn, 16));
      mn = fminf(mn, __shfl_xor(mn, 32));
      f32x4 ep, en;
      float sp = 0.f, sn = 0.f;
#pragma unroll
      for (int j = 0; j < 4; ++j) {
        ep[j] = __expf(pk[j] - mx); sp += ep[j];
        en[j] = __expf(mn - pk[j]); sn += en[j];
      }
      sp = red_g(sp);
      sn = red_g(sn);
      const float ip = __builtin_amdgcn_rcpf(sp), in_ = __builtin_amdgcn_rcpf(sn);
#pragma unroll
      for (int j = 0; j < 4; ++j) { ep[j] *= ip; en[j] *= in_; }
      *(bf16x4*)(phs + (16 * t + r) * 64 + 8 * g) = cvt4(ep);
      *(bf16x4*)(phs + (16 * t + r) * 64 + 32 + 8 * g) = cvt4(en);
    }
    const bf16x8 vB = __builtin_shufflevector(vb[0], vb[1], 0, 1, 2, 3, 4, 5, 6, 7);
    float* rd = red + w * KVP;
#pragma unroll
    for (int fh = 0; fh < 2; ++fh) {
      const int o0 = (4 * g + q) * 64 + fh * 32 + p4 * 8;
      bf16x8 pa = tr_frag(phs, o0, o0 + 1024);
      f32x4 kvv = mfma16(pa, vB, zf);
      f32x4 zz = mfma16(pa, ones, zf);
#pragma unroll
      for (int j = 0; j < 4; ++j) rd[(16 * fh + 4 * g + j) * 16 + r] = kvv[j];
      if (r == 0) *(f32x4*)(rd + 512 + 16 * fh + 4 * g) = zz;
    }
    bar();
    if (tid < 2 * KVP / 4) {
      const int h2 = tid / (KVP / 4), i4 = tid % (KVP / 4);
      f32x4 sum = *(const f32x4*)(red + h2 * KVP + 4 * i4);
#pragma unroll
      for (int ww = 1; ww < 4; ++ww) sum += *(const f32x4*)(red + (ww * 2 + h2) * KVP + 4 * i4);
      *(f32x4*)(kvp + ((size_t)(nt * 2 + h2) * 16 + rbk) * KVP + 4 * i4) = sum;
    }
  }
}

__global__ __launch_bounds__(256, 1) void attn_apply(const u16* __restrict__ pq, const float* __restrict__ kvp,
                                                     u16* __restrict__ o) {
  __shared__ __attribute__((aligned(16))) __bf16 kvT[16 * 32 + 32];
  const int b = blockIdx.x;
  const int chunk = b & 7, head = b >> 3;
  const int tid = threadIdx.x, w = tid >> 6, l = tid & 63, r = l & 15, g = l >> 4;
  const int row0 = chunk * 256 + w * 64 + r;
  bf16x8 pf[4];
#pragma unroll
  for (int t = 0; t < 4; ++t)
    pf[t] = *(const bf16x8*)(pq + (size_t)(row0 + 16 * t) * DM + head * 16 + 8 * (g & 1));
  if (tid < KVP / 4) {
    const float* p = kvp + (size_t)head * 16 * KVP + 4 * tid;
    f32x4 v[16];
#pragma unroll
    for (int i = 0; i < 16; ++i) v[i] = *(const f32x4*)(p + i * KVP);
    f32x4 s = v[0];
#pragma unroll
    for (int i = 1; i < 16; ++i) s += v[i];
    if (tid < 128) {
      const int f = tid >> 2, d0 = (tid & 3) * 4;
#pragma unroll
      for (int j = 0; j < 4; ++j) kvT[(d0 + j) * 32 + f] = (__bf16)s[j];
    } else {
      const int f0 = (tid - 128) * 4;
#pragma unroll
      for (int j = 0; j < 4; ++j) kvT[512 + f0 + j] = (__bf16)s[j];
    }
  }
  bf16x8 ph[4];
  const float sg = (g < 2) ? 1.0f : -1.0f;
#pragma unroll
  for (int t = 0; t < 4; ++t) {
    float v[8];
#pragma unroll
    for (int j = 0; j < 8; ++j) v[j] = sg * (float)pf[t][j];
    float mx = v[0];
#pragma unroll
    for (int j = 1; j < 8; ++j) mx = fmaxf(mx, v[j]);
    mx = fmaxf(mx, __shfl_xor(mx, 16));
    float s = 0.f;
#pragma unroll
    for (int j = 0; j < 8; ++j) { v[j] = __expf(v[j] - mx); s += v[j]; }
    s += __shfl_xor(s, 16);
    const float is = __builtin_amdgcn_rcpf(s);
#pragma unroll
    for (int j = 0; j < 8; ++j) ph[t][j] = (__bf16)(v[j] * is);
  }
  __syncthreads();
  const bf16x8 kvA = *(const bf16x8*)(kvT + r * 32 + 8 * g);
  const bf16x8 zA = *(const bf16x8*)(kvT + 512 + 8 * g);
  const f32x4 zf = {0.f, 0.f, 0.f, 0.f};
#pragma unroll
  for (int t = 0; t < 4; ++t) {
    f32x4 num = mfma16(kvA, ph[t], zf);
    f32x4 den = mfma16(zA, ph[t], zf);
    const float dn = bfr(bfr(den[0]) + 1e-6f);
    f32x4 v;
#pragma unroll
    for (int j = 0; j < 4; ++j) v[j] = bfr(num[j]) / dn;
    *(bf16x4*)(o + (size_t)(row0 + 16 * t) * DM + head * 16 + 4 * g) = cvt4(v);
  }
}

extern "C" void kernel_launch(void* const* d_in, const int* in_sizes, int n_in, void* d_out, int out_size,
                              void* d_ws, size_t ws_size, hipStream_t stream) {
  const u16* x0 = (const u16*)d_in[0];
  const u16* wqkv = (const u16*)d_in[1];
  const u16* wphi = (const u16*)d_in[2];
  const u16* wo = (const u16*)d_in[3];
  const u16* w1 = (const u16*)d_in[5];
  const u16* w2 = (const u16*)d_in[6];
  const u16* w3 = (const u16*)d_in[7];
  const u16* w4 = (const u16*)d_in[8];
  char* ws = (char*)d_ws;
  const size_t T = (size_t)SEQ * DM * 2;
  u16* pq = (u16*)(ws + 0 * T);
  u16* o = (u16*)(ws + 1 * T);
  u16* x1 = (u16*)(ws + 2 * T);
  u16* xn = (u16*)(ws + 3 * T);
  u16* y1 = (u16*)(ws + 4 * T);
  u16* x2 = (u16*)(ws + 5 * T);
  u16* h = (u16*)(ws + 6 * T + 4096);
  u16* h2 = (u16*)(ws + 8 * T);
  float* kvp = (float*)(ws + 11 * T);

  for (int layer = 0; layer < 2; ++layer) {
    const u16* xin = layer == 0 ? x0 : x2;
    u16* xout = layer == 0 ? x2 : (u16*)d_out;
    gemm_l<0><<<256, 512, 0, stream>>>(xin, wqkv, wphi, pq, kvp);
    attn_apply<<<256, 256, 0, stream>>>(pq, kvp, o);
    gemm_s<0><<<256, 512, 0, stream>>>(o, wo, xin, x1, nullptr);
    gemm_s<1><<<256, 512, 0, stream>>>(x1, w1, nullptr, h, xn);
    gemm_s<2><<<256, 512, 0, stream>>>(h, w2, xn, y1, nullptr);
    gemm_l<1><<<240, 512, 0, stream>>>(y1, w3, nullptr, h2, nullptr);
    gemm_b4<<<256, 512, 0, stream>>>(h2, w4, y1, x1, xout);
  }
}
```

```cpp
#include <hip/hip_runtime.h>
#include <stdint.h>

typedef __attribute__((ext_vector_type(8))) __bf16 bf16x8;
typedef __attribute__((ext_vector_type(4))) __bf16 bf16x4;
typedef __attribute__((ext_vector_type(2))) __bf16 bf16x2;
typedef __attribute__((ext_vector_type(4))) float f32x4;
typedef __attribute__((ext_vector_type(2))) float f32x2;
typedef __attribute__((ext_vector_type(4))) unsigned u32x4;
typedef __attribute__((ext_vector_type(2))) unsigned u32x2;
typedef unsigned short u16;

#define LDSP(T, p) ((__attribute__((address_space(3))) T*)(p))
#define SB __builtin_amdgcn_sched_barrier(0);

#define ST(i)

static constexpr int SEQ = 2048;
static constexpr int DM = 512;
static constexpr int FF = 1376;
static constexpr unsigned KB = 1024;
static constexpr unsigned WS_CS = 64 * KB;
static constexpr unsigned WS_XT0 = 1u << 20;
static constexpr unsigned WS_L0 = 4u << 20, WS_LSTR = 20u << 20;
static constexpr unsigned O_KVP = 0, O_O = 2u << 20, O_X1 = 4u << 20, O_H = 6u << 20, O_Y1 = 8u << 20,
                          O_X2 = 10u << 20, O_H2 = 12u << 20;
static constexpr unsigned KVREC = 4352;
static constexpr int SCR = 140 * 1024;
static constexpr int SLOT = 2176;
static constexpr int L_CS = SCR + 8 * SLOT;
static constexpr int L_PHI = L_CS + 1024;
#define SPINCAP 400000u
static constexpr unsigned SPIN_CAP = SPINCAP;

__device__ __forceinline__ f32x4 mfma16(bf16x8 a, bf16x8 b, f32x4 c) {
  return __builtin_amdgcn_mfma_f32_16x16x32_bf16(a, b, c, 0, 0, 0);
}
__device__ __forceinline__ bf16x4 tr4(const char* smem, int off) {
  return __builtin_amdgcn_ds_read_tr16_b64_v4bf16(LDSP(bf16x4, smem + off));
}
__device__ __forceinline__ bf16x8 tr_frag(const char* smem, int off0, int off1) {
  bf16x4 v0 = tr4(smem, off0);
  bf16x4 v1 = tr4(smem, off1);
  return __builtin_shufflevector(v0, v1, 0, 1, 2, 3, 4, 5, 6, 7);
}
__device__ __forceinline__ void glds16(const void* base, unsigned voff, unsigned lds) {
  unsigned keep;
  asm volatile("s_mov_b32 %0, m0\n\ts_mov_b32 m0, %3\n\ts_nop 0\n\tglobal_load_lds_dwordx4 %1, %2\n\ts_mov_b32 m0, %0"
               : "=&s"(keep) : "v"(voff), "s"(base), "s"(lds) : "memory");
}
__device__ __forceinline__ unsigned lds_addr(const char* p) { return (unsigned)(uintptr_t)LDSP(const char, p); }
template <int N>
__device__ __forceinline__ void wait_vm() {
  asm volatile("s_waitcnt vmcnt(%0)" ::"n"(N) : "memory");
}
template <int N>
__device__ __forceinline__ void wait_vm(u32x4& a) {
  asm volatile("s_waitcnt vmcnt(%1)" : "+v"(a) : "n"(N) : "memory");
}
__device__ __forceinline__ void bar() {
  asm volatile("s_waitcnt lgkmcnt(0)" ::: "memory");
  __builtin_amdgcn_s_barrier();
  asm volatile("" ::: "memory");
}
template <int IMM>
__device__ __forceinline__ void bload(u32x4& d, unsigned voff, u32x4 rs, unsigned soff) {
  asm volatile("buffer_load_dwordx4 %0, %1, %2, %3 offen offset:%4 sc1"
               : "=v"(d) : "v"(voff), "s"(rs), "s"(soff), "n"(IMM) : "memory");
}
__device__ __forceinline__ void bstore(u32x4 d, unsigned voff, __amdgpu_buffer_rsrc_t rs, unsigned soff) {
  __builtin_amdgcn_raw_buffer_store_b128(d, rs, voff, soff, 16);
  asm volatile("s_nop 1" ::"v"(d) : "memory");
}
__device__ __forceinline__ u32x4 mk_rsrc(const void* p, unsigned bytes) {
  const unsigned long long a = (unsigned long long)p;
  u32x4 r;
  r[0] = (unsigned)a; r[1] = (unsigned)(a >> 32) & 0xffffu; r[2] = bytes; r[3] = 0x00020000u;
  return r;
}
__device__ __forceinline__ bf16x8 asb(u32x4 v) { return __builtin_bit_cast(bf16x8, v); }
__device__ __forceinline__ u32x4 asu(bf16x8 v) { return __builtin_bit_cast(u32x4, v); }
__device__ __forceinline__ float dot8(bf16x8 a, bf16x8 b, float c) {
  c = __builtin_amdgcn_fdot2_f32_bf16(__builtin_shufflevector(a, a, 0, 1), __builtin_shufflevector(b, b, 0, 1), c, false);
  c = __builtin_amdgcn_fdot2_f32_bf16(__builtin_shufflevector(a, a, 2, 3), __builtin_shufflevector(b, b, 2, 3), c, false);
  c = __builtin_amdgcn_fdot2_f32_bf16(__builtin_shufflevector(a, a, 4, 5), __builtin_shufflevector(b, b, 4, 5), c, false);
  c = __builtin_amdgcn_fdot2_f32_bf16(__builtin_shufflevector(a, a, 6, 7), __builtin_shufflevector(b, b, 6, 7), c, false);
  return c;
}
__device__ __forceinline__ float bfr(float x) { return (float)(__bf16)x; }
__device__ __forceinline__ bf16x4 cvt4(f32x4 v) {
  bf16x4 r;
  r[0] = (__bf16)v[0]; r[1] = (__bf16)v[1]; r[2] = (__bf16)v[2]; r[3] = (__bf16)v[3];
  return r;
}
__device__ __forceinline__ f32x4 up4(bf16x4 v) {
  f32x4 r;
  r[0] = (float)v[0]; r[1] = (float)v[1]; r[2] = (float)v[2]; r[3] = (float)v[3];
  return r;
}
__device__ __forceinline__ f32x4 bfr4(f32x4 v) { return up4(cvt4(v)); }
__device__ __forceinline__ bf16x4 lo4(bf16x8 v) { return __builtin_shufflevector(v, v, 0, 1, 2, 3); }
__device__ __forceinline__ bf16x4 hi4(bf16x8 v) { return __builtin_shufflevector(v, v, 4, 5, 6, 7); }
__device__ __forceinline__ bf16x8 cat8(bf16x4 a, bf16x4 b) { return __builtin_shufflevector(a, b, 0, 1, 2, 3, 4, 5, 6, 7); }
__device__ __forceinline__ bf16x8 pad8(bf16x4 v) {
  bf16x4 z = {(__bf16)0.f, (__bf16)0.f, (__bf16)0.f, (__bf16)0.f};
  return cat8(v, z);
}
__device__ __forceinline__ bf16x8 ones8() {
  bf16x8 r;
#pragma unroll
  for (int i = 0; i < 8; ++i) r[i] = (__bf16)1.0f;
  return r;
}
__device__ __forceinline__ float gelu_t(float x) {
  const float c1 = -2.0f * 0.7978845608028654f * 1.4426950408889634f;
  const float c2 = c1 * 0.044715f;
  const float t = x * __builtin_fmaf(x * x, c2, c1);
  const float e = __builtin_amdgcn_exp2f(t);
  return x * __builtin_amdgcn_rcpf(1.0f + e);
}
__device__ __forceinline__ float xg_add(float v) {
  unsigned x = __builtin_bit_cast(unsigned, v);
  auto r = __builtin_amdgcn_permlane16_swap(x, x, false, false);
  v = __builtin_bit_cast(float, (unsigned)r[0]) + __builtin_bit_cast(float, (unsigned)r[1]);
  x = __builtin_bit_cast(unsigned, v);
  auto r2 = __builtin_amdgcn_permlane32_swap(x, x, false, false);
  return __builtin_bit_cast(float, (unsigned)r2[0]) + __builtin_bit_cast(float, (unsigned)r2[1]);
}
__device__ __forceinline__ float xg_max(float v) {
  unsigned x = __builtin_bit_cast(unsigned, v);
  auto r = __builtin_amdgcn_permlane16_swap(x, x, false, false);
  v = fmaxf(__builtin_bit_cast(float, (unsigned)r[0]), __builtin_bit_cast(float, (unsigned)r[1]));
  x = __builtin_bit_cast(unsigned, v);
  auto r2 = __builtin_amdgcn_permlane32_swap(x, x, false, false);
  return fmaxf(__builtin_bit_cast(float, (unsigned)r2[0]), __builtin_bit_cast(float, (unsigned)r2[1]));
}
__device__ __forceinline__ float half_add(float v) {
  unsigned x = __builtin_bit_cast(unsigned, v);
  auto r2 = __builtin_amdgcn_permlane32_swap(x, x, false, false);
  return __builtin_bit_cast(float, (unsigned)r2[0]) + __builtin_bit_cast(float, (unsigned)r2[1]);
}
template <int N>
__device__ __forceinline__ float ror_add(float v) {
  int x = __builtin_bit_cast(int, v);
  int y = __builtin_amdgcn_update_dpp(x, x, 0x120 + N, 0xf, 0xf, false);
  return v + __builtin_bit_cast(float, y);
}
__device__ __forceinline__ void dual_softmax(f32x4 p, bf16x4& pl, bf16x4& mi) {
  const float L2E = 1.4426950408889634f;
  p = bfr4(p);
  float mx = xg_max(fmaxf(fmaxf(p[0], p[1]), fmaxf(p[2], p[3])));
  float nm = xg_max(fmaxf(fmaxf(-p[0], -p[1]), fmaxf(-p[2], -p[3])));
  f32x4 ep, en;
#pragma unroll
  for (int j = 0; j < 4; ++j) {
    ep[j] = __builtin_amdgcn_exp2f((p[j] - mx) * L2E);
    en[j] = __builtin_amdgcn_exp2f((-p[j] - nm) * L2E);
  }
  const float sp = xg_add((ep[0] + ep[1]) + (ep[2] + ep[3]));
  const float sn = xg_add((en[0] + en[1]) + (en[2] + en[3]));
  pl = cvt4(ep * __builtin_amdgcn_rcpf(sp));
  mi = cvt4(en * __builtin_amdgcn_rcpf(sn));
}
__device__ __forceinline__ int opq(int v) { asm volatile("" : "+v"(v)); return v; }
__device__ __forceinline__ bf16x8 opq8(bf16x8 v) { u32x4 u = asu(v); asm volatile("" : "+v"(u)); return asb(u); }
#define LANE_VARS                                                                                  \
  const int l = opq(l0), r = l & 15, g = l >> 4, q = r >> 2, p4 = l & 3;                           \
  const unsigned l16 = (unsigned)l * 16;                                                           \
  const int woffL = (4 * g + q) * 128 + ((2 * (g & 1) + (q >> 1)) << 5) + p4 * 8;                  \
  const int woffS = woffL ^ (kh << 6);                                                             \
  const int w96 = (4 * g + q) * 192 + p4 * 8;                                                      \
  const int w96E = w96 + (g & 1) * 32, w96O = w96 - (g & 1) * 32;                                  \
  (void)r; (void)l16; (void)woffS; (void)w96E; (void)w96O;
__device__ __forceinline__ void flag_store(const char* ws, unsigned off) {
  unsigned one = 1u;
  asm volatile("global_store_dword %0, %1, %2 sc1" ::"v"(off), "v"(one), "s"(ws) : "memory");
}
__device__ __forceinline__ void poll_issue(unsigned& v, const char* ws, unsigned off) {
  asm volatile("global_load_dword %0, %1, %2 sc1" : "=v"(v) : "v"(off), "s"(ws) : "memory");
}
__device__ __forceinline__ void poll_finish(unsigned v, const char* ws, unsigned off) {
  asm volatile("s_waitcnt vmcnt(0)" : "+v"(v)::"memory");
  unsigned spins = 0;
  while (!__all(v != 0u)) {
    if (++spins > SPIN_CAP) break;
    asm volatile("global_load_dword %0, %1, %2 sc1\n\ts_waitcnt vmcnt(0)" : "=v"(v) : "v"(off), "s"(ws) : "memory");
  }
}
__device__ __forceinline__ unsigned poll_off(int arr, int base, int n, int l) {
  int i = base + (l < n ? l : n - 1);
  i = i < 0 ? 0 : (i > 255 ? 255 : i);
  return (unsigned)(arr * 1024 + i * 4);
}

__global__ __launch_bounds__(256) void setup_k(const u16* __restrict__ x0, const u16* __restrict__ wqkv,
                                               const u16* __restrict__ w1, const u16* __restrict__ w3,
                                               char* __restrict__ ws) {
  __shared__ float red[4][16];
  const int b = blockIdx.x, tid = threadIdx.x;
#pragma unroll
  for (int it = 0; it < 2; ++it) {
    const int o = (it * 256 + b) * 256 + tid;
    const int l = o & 63, blk = o >> 6, cg = blk & 15, T = blk >> 4, r = l & 15, g = l >> 4;
    const u16* src = x0 + (size_t)(T * 16 + r) * DM + cg * 32 + 4 * g;
    const u32x2 lo = *(const u32x2*)src, hi = *(const u32x2*)(src + 16);
    *(u32x4*)(ws + WS_XT0 + (size_t)o * 16) = (u32x4){lo[0], lo[1], hi[0], hi[1]};
  }
  if (b >= 214 && b < 228) ((unsigned*)ws)[(b - 214) * 256 + tid] = 0u;
  if (b < 214) {
    const u16* W; int ldw, c0; float* dst;
    float* cs = (float*)(ws + WS_CS);
    if (b < 96) {
      W = wqkv; ldw = 1536; c0 = b * 16;
      const int part = c0 >> 9, wi = c0 & 511;
      dst = cs + (wi >> 5) * 96 + (part * 2 + ((wi >> 4) & 1)) * 16;
    } else if (b < 128) {
      W = w1; ldw = 512; c0 = (b - 96) * 16; dst = cs + 1536 + c0;
    } else {
      W = w3; ldw = FF; c0 = (b - 128) * 16; dst = cs + 2048 + c0;
    }
    const u16* p = W + (size_t)(tid >> 1) * 4 * ldw + c0 + (tid & 1) * 8;
    bf16x8 v[4];
#pragma unroll
    for (int i = 0; i < 4; ++i) v[i] = *(const bf16x8*)(p + (size_t)i * ldw);
    float s[8];
#pragma unroll
    for (int j = 0; j < 8; ++j) s[j] = ((float)v[0][j] + (float)v[1][j]) + ((float)v[2][j] + (float)v[3][j]);
#pragma unroll
    for (int m = 2; m < 64; m <<= 1)
#pragma unroll
      for (int j = 0; j < 8; ++j) s[j] += __shfl_xor(s[j], m);
    if ((tid & 63) < 2) {
#pragma unroll
      for (int j = 0; j < 8; ++j) red[tid >> 6][(tid & 1) * 8 + j] = s[j];
    }
    __syncthreads();
    if (tid < 16) dst[tid] = (red[0][tid] + red[1][tid]) + (red[2][tid] + red[3][tid]);
  }
}

template <int P0, int P1A, int SPLIT, int P1B, bool STATS>
__device__ __forceinline__ void s_loop(const char* smem, int woff, int kh, u32x4 (&a)[8], f32x4 (&acc)[4],
                                       float& ssum, float& ssq) {
  constexpr int NWIN = 5;
  const int bA = woff + (kh ? P1A : P0) * 1024;
  const int bB = woff + (kh ? P1B : P0 + 4 * SPLIT) * 1024;
  int oA[4], oB[4];
#pragma unroll
  for (int t = 0; t < 4; ++t) { oA[t] = bA ^ (t << 5); oB[t] = bB ^ (t << 5); }
  bf16x8 wf[8][4];
  const bf16x8 ones = ones8();
#define S_RD(i)                                                                              \
  _Pragma("unroll") for (int t = 0; t < 4; ++t) {                                            \
    const int o_ = ((i) < SPLIT) ? oA[t] + (i) * 4096 : oB[t] + ((i) - SPLIT) * 4096;        \
    wf[i][t] = tr_frag(smem, o_, o_ + 2048);                                                 \
  }
#pragma unroll
  for (int i = 0; i < NWIN; ++i) { S_RD(i) }
#pragma unroll
  for (int t = 0; t < 4; ++t) acc[t] = (f32x4){0.f, 0.f, 0.f, 0.f};
#define S_STEP(i)                                                                            \
  {                                                                                          \
    wait_vm<7 - (i)>(a[i]);                                                                  \
    const bf16x8 af = asb(a[i]);                                                             \
    _Pragma("unroll") for (int t = 0; t < 4; ++t) acc[t] = mfma16(wf[i][t], af, acc[t]);     \
    if constexpr (STATS) { ssum = dot8(af, ones, ssum); ssq = dot8(af, af, ssq); }           \
    if constexpr ((i) + NWIN < 8) { S_RD(((i) + NWIN) & 7) }                                 \
    SB                                                                                       \
  }
  S_STEP(0) S_STEP(1) S_STEP(2) S_STEP(3) S_STEP(4) S_STEP(5) S_STEP(6) S_STEP(7)
#undef S_STEP
#undef S_RD
}

__global__ __launch_bounds__(640, 1) void mega_k(const u16* __restrict__ wqkv, const u16* __restrict__ wphi,
                                                 const u16* __restrict__ wo, const u16* __restrict__ w1,
                                                 const u16* __restrict__ w2, const u16* __restrict__ w3,
                                                 const u16* __restrict__ w4, char* __restrict__ ws,
                                                 u16* __restrict__ out) {
  __shared__ __attribute__((aligned(1024))) char smem[163840];
  const int b = blockIdx.x;
  const int xcd = b & 7, idx = b >> 3;
  const int mt = xcd * 4 + (idx >> 3), nt = idx & 7;
  const int rb = xcd * 2 + (idx >> 4), hp = idx & 15;
  const int tid = threadIdx.x, w = __builtin_amdgcn_readfirstlane(tid >> 6), l0 = tid & 63;
  const unsigned lb = lds_addr(smem);
#define KV_LANE                                                   \
  const int lr = l >> 3;                                          \
  const int lu64 = ((l & 7) >> 1) ^ ((lr >> 1) & 3);              \
  const unsigned vkv = (unsigned)(lr * 1536 + 512 + (lu64 >> 1) * 512 + hp * 32 + (lu64 & 1) * 16 + (l & 1) * 8) * 2;

  if (w >= 8) {
    const int ld = w - 8;
    const int l = opq(l0);
    KV_LANE
    const unsigned v64 = (unsigned)(lr * 512 + nt * 64 + lu64 * 16 + (l & 1) * 8) * 2;
    const int qr = l >> 2;
    const unsigned vq = (unsigned)(qr * 1536 + hp * 32 + ((((l & 3) >> 1) ^ ((qr >> 2) & 1)) * 16) + (l & 1) * 8) * 2;
    unsigned v3[3];
#pragma unroll
    for (int bb = 0; bb < 3; ++bb) {
      const int s16 = 64 * bb + l;
      const int kk = s16 / 12, j = s16 % 12;
      const int lu = (j >> 1) ^ ((kk >> 2) & 1);
      int col = hp * 96 + lu * 16 + (j & 1) * 8;
      col = col < FF ? col : (col & 31);
      v3[bb] = (unsigned)(kk * FF + col) * 2;
    }
    auto run64 = [&](const u16* W, int ldw, unsigned vo, int p0, int n, int pos) {
      const char* gp = (const char*)W + (size_t)(p0 + ld) * 16 * ldw;
      unsigned la = lb + (unsigned)(pos + ld) * 1024;
#pragma unroll 1
      for (int i = 0; i < n; ++i) {
        glds16(gp, vo, la);
        gp += (size_t)32 * ldw;
        la += 2048;
      }
    };
    auto runq = [&]() {
      const char* gp = (const char*)wqkv + (size_t)ld * 32 * 1536;
      unsigned la = lb + (unsigned)(64 + ld) * 1024;
#pragma unroll 1
      for (int i = 0; i < 16; ++i) {
        glds16(gp, vq, la);
        gp += (size_t)64 * 1536;
        la += 2048;
      }
    };
    auto run96 = [&](int g0, int n) {
      const char* gp = (const char*)w3 + (size_t)(g0 + ld) * 32 * FF;
      unsigned la = lb + 8 * 1024 + (unsigned)(g0 + ld) * 3072;
#pragma unroll 1
      for (int i = 0; i < n; ++i) {
        glds16(gp, v3[0], la);
        glds16(gp, v3[1], la + 1024);
        glds16(gp, v3[2], la + 2048);
        gp += (size_t)64 * FF;
        la += 6144;
      }
    };
    auto run4 = [&](int j0, int n) {
#pragma unroll 1
      for (int i = 0; i < n; ++i) {
        const int j = j0 + 2 * i;
        const int ks = (j >> 1) + (j & 1) * 22;
        const char* gp = (const char*)w4 + (size_t)ks * 32768;
        const unsigned la = lb + (unsigned)(j < 9 ? 104 + 4 * j : 4 * (j - 9)) * 1024;
        glds16(gp, v64, la);
        glds16(gp + 8192, v64, la + 1024);
        glds16(gp + 16384, v64, la + 2048);
        glds16(gp + 24576, v64, la + 3072);
      }
    };
    if (ld == 0) {
      const unsigned vo = l < 24 ? hp * 384 + l * 16 : (l < 40 ? 6144 + nt * 256 + (l - 24) * 16 : 8192 + hp * 384 + (l - 40) * 16);
      glds16(ws + WS_CS, vo, lb + L_CS);
    } else {
      glds16(wphi + hp * 512, l * 16, lb + L_PHI);
    }
    runq();
    run64(wo, 512, v64, 0, 22, 96);
#pragma unroll 1
    for (int layer = 0; layer < 2; ++layer) {
      wait_vm<46>(); bar();
      if (layer == 0) bar();
      wait_vm<38>(); bar();
      run64(w1, 512, v64, 4, 4, 24);
      bar();
      run64(w1, 512, v64, 12, 16, 32);
      wait_vm<42>(); bar();
      run64(wo, 512, v64, 44, 10, 0);
      run64(w1, 512, v64, 0, 2, 20);
      bar();
      bar();
      run64(w1, 512, v64, 44, 10, 64);
      run64(w2, 512, v64, 0, 6, 84);
      bar();
      wait_vm<18>(); bar();
      bar();
      run64(w2, 512, v64, 12, 16, 96);
      bar();
      run64(w2, 512, v64, 44, 6, 128);
      run64(w2, 512, v64, 56, 4, 0);
      run96(0, 2);
      wait_vm<38>(); bar();
      bar();
      run96(4, 5);
      bar();
      run96(14, 5);
      wait_vm<36>(); bar();
      bar();
      run96(24, 4);
      run4(ld, 1);
      bar();
      run4(2 + ld, 4);
      {
        const char* gp = (const char*)w4 + 5 * 32768 + ld * 16384;
        glds16(gp, v64, lb + 4096 + ld * 2048);
        glds16(gp + 8192, v64, lb + 4096 + ld * 2048 + 1024);
      }
      wait_vm<34>(); bar();
      wait_vm<22>(); bar();
      run4(12 - ld, 6);
      bar();
      run4(24 - ld, 6);
      bar();
      wait_vm<48>(); bar();
      wait_vm<0>(); bar();
      run4(36 - ld, 4);
      wait_vm<0>(); bar();
      if (layer == 0) run64(wqkv, 1536, vkv, 0, 24, 0);
      bar();
      if (layer == 0) { run64(wqkv, 1536, vkv, 48, 8, 48); runq(); }
      bar();
      if (layer == 0) run64(wo, 512, v64, 0, 22, 96);
    }
    return;
  }

  const int rt = w & 3, kh = w >> 2;
  const u32x4 rs = mk_rsrc(ws, 0x10000000u);
  const __amdgpu_buffer_rsrc_t rst = __builtin_amdgcn_make_buffer_rsrc(ws, 0, 0x10000000, 0x00020000);
  const unsigned srow = (unsigned)(mt * 4 + rt);
  const unsigned lrow = (unsigned)(rb * 8 + w);
  const unsigned sblk = (srow * 16 + nt * 2 + kh) * 1024;
  char* xs = smem + SCR + w * SLOT;
  const char* xp = smem + SCR + (w ^ 4) * SLOT;
  const float* csl = (const float*)(smem + L_CS);
  const f32x4 zf = {0.f, 0.f, 0.f, 0.f};
  const bf16x8 ones = ones8();

  {
    const int l = opq(l0);
    KV_LANE
    const char* gp = (const char*)wqkv + (size_t)w * 8 * 16 * 1536;
#pragma unroll
    for (int i = 0; i < 8; ++i) glds16(gp + (size_t)i * 16 * 1536, vkv, lb + (unsigned)(w * 8 + i) * 1024);
  }

#pragma unroll 1
  for (int layer = 0; layer < 2; ++layer) {
    const unsigned lbase = WS_L0 + (unsigned)layer * WS_LSTR;
    const unsigned xin = layer == 0 ? WS_XT0 : WS_L0 + O_X2;
    const int fa = layer * 7;
    bf16x8 phq[2];
    ST(0)
    {
      LANE_VARS
      bar();
      ST(1)
      u32x4 a[16];
      {
        const unsigned so = xin + lrow * 16384;
#define LDA(i) bload<((i) & 3) * 1024>(a[i], l16, rs, so + ((i) >> 2) * 4096);
        LDA(0) LDA(1) LDA(2) LDA(3) LDA(4) LDA(5) LDA(6) LDA(7)
        LDA(8) LDA(9) LDA(10) LDA(11) LDA(12) LDA(13) LDA(14) LDA(15)
#undef LDA
      }
      if (layer == 0) { wait_vm<16>(); bar(); }
      int o4[4];
#pragma unroll
      for (int t = 0; t < 4; ++t) o4[t] = woffL ^ (t << 5);
      bf16x8 wf[16][4];
      f32x4 acc[4];
      float ssum = 0.f, ssq = 0.f;
#define P1_RD(i) _Pragma("unroll") for (int t = 0; t < 4; ++t) wf[i][t] = tr_frag(smem, o4[t] + (i) * 4096, o4[t] + (i) * 4096 + 2048);
      P1_RD(0) P1_RD(1) P1_RD(2)
#pragma unroll
      for (int t = 0; t < 4; ++t) acc[t] = zf;
#define P1_STEP(i)                                                                        \
  {                                                                                       \
    wait_vm<15 - (i)>(a[i]);                                                              \
    const bf16x8 af = asb(a[i]);                                                          \
    _Pragma("unroll") for (int t = 0; t < 4; ++t) acc[t] = mfma16(wf[i][t], af, acc[t]);  \
    ssum = dot8(af, ones, ssum); ssq = dot8(af, af, ssq);                                 \
    if constexpr ((i) == 5) { SB bar(); }                                      \
    if constexpr ((i) + 3 < 16) { P1_RD(((i) + 3) & 15) }                                 \
    SB                                                                                    \
  }
      P1_STEP(0) P1_STEP(1) P1_STEP(2) P1_STEP(3) P1_STEP(4) P1_STEP(5) P1_STEP(6) P1_STEP(7)
      P1_STEP(8) P1_STEP(9) P1_STEP(10) P1_STEP(11) P1_STEP(12) P1_STEP(13) P1_STEP(14) P1_STEP(15)
#undef P1_STEP
#undef P1_RD
      ST(2)
      ssum = xg_add(ssum); ssq = xg_add(ssq);
      const float mean = ssum * (1.0f / DM);
      float var = ssq * (1.0f / DM) - mean * mean;
      var = var < 0.f ? 0.f : var;
      const float rstd = __builtin_amdgcn_rsqf(var + 1e-5f);
      bf16x8 wph[2];
#pragma unroll
      for (int h = 0; h < 2; ++h) wph[h] = pad8(tr4(smem, L_PHI + h * 512 + (4 * g + q) * 32 + p4 * 8));
      {
        char* trow = smem + (16 * w + r) * 192 + g * 8;
        const int sw = (q & 1) << 5;
#pragma unroll
        for (int h = 0; h < 2; ++h) {
          const f32x4 ck = *(const f32x4*)(csl + (2 + h) * 16 + 4 * g);
          const f32x4 cv = *(const f32x4*)(csl + (4 + h) * 16 + 4 * g);
          const f32x4 kk = (acc[h] - mean * ck) * rstd;
          const f32x4 vv = (acc[2 + h] - mean * cv) * rstd;
          *(bf16x4*)(trow + (((4 + h) << 5) ^ sw)) = cvt4(vv);
          const f32x4 pk = mfma16(wph[h], pad8(cvt4(kk)), zf);
          bf16x4 pl, mi;
          dual_softmax(pk, pl, mi);
          *(bf16x4*)(trow + (((2 * h) << 5) ^ sw)) = pl;
          *(bf16x4*)(trow + (((2 * h + 1) << 5) ^ sw)) = mi;
        }
      }
      ST(3)
      bar();
      {
        const int u = w & 3;
        const int oa = ((u & 1) ? w96O : w96E) + u * 32;
        const int ub = 4 + (u >> 1);
        const int ob = ((ub & 1) ? w96O : w96E) + ub * 32;
        f32x4 kv = zf;
        const unsigned rec = lbase + O_KVP + (unsigned)(hp * 16 + rb) * KVREC;
        if (kh == 0) {
#pragma unroll
          for (int s = 0; s < 4; ++s) {
            const bf16x8 pa = tr_frag(smem, oa + s * 6144, oa + s * 6144 + 3072);
            const bf16x8 vb = tr_frag(smem, ob + s * 6144, ob + s * 6144 + 3072);
            kv = mfma16(pa, vb, kv);
          }
          bstore(__builtin_bit_cast(u32x4, kv), l16, rst, rec + u * 1024);
        } else {
#pragma unroll
          for (int s = 0; s < 4; ++s) {
            const bf16x8 pa = tr_frag(smem, oa + s * 6144, oa + s * 6144 + 3072);
            kv = mfma16(pa, ones, kv);
          }
          if (r == 0) bstore(__builtin_bit_cast(u32x4, kv), (unsigned)g * 16, rst, rec + 4096 + u * 64);
        }
      }
      wait_vm<0>();
      ST(4)
      bar();
      unsigned pv = 1u;
      const unsigned po = poll_off(fa + 0, hp * 16, 16, l);
      if (w == 0) {
        if (l == 0) flag_store(ws, (unsigned)((fa + 0) * 1024 + (hp * 16 + rb) * 4));
        poll_issue(pv, ws, po);
      }
      f32x4 aq[2] = {zf, zf};
      {
        const int oq = 65536 + (4 * g + q) * 64 + p4 * 8;
        const int oq0 = oq + ((g & 1) << 5), oq1 = oq + (((g & 1) ^ 1) << 5);
#pragma unroll
        for (int i = 0; i < 16; ++i) {
          const bf16x8 f0 = tr_frag(smem, oq0 + i * 2048, oq0 + i * 2048 + 1024);
          const bf16x8 f1 = tr_frag(smem, oq1 + i * 2048, oq1 + i * 2048 + 1024);
          aq[0] = mfma16(f0, asb(a[i]), aq[0]);
          aq[1] = mfma16(f1, asb(a[i]), aq[1]);
        }
      }
      if (w == 0) poll_finish(pv, ws, po);
      ST(5)
      bar();
      ST(6)
      u32x4 pt[8], pz;
      {
        const unsigned vo = (unsigned)(l >> 5) * 8 * KVREC + (unsigned)(kh * 32 + (l & 31)) * 16;
        const unsigned so = lbase + O_KVP + (unsigned)hp * 16 * KVREC;
#pragma unroll
        for (int i = 0; i < 8; ++i) bload<0>(pt[i], vo, rs, so + i * KVREC + rt * 1024);
        bload<0>(pz, (unsigned)r * KVREC + (unsigned)g * 16, rs, so + 4096 + rt * 64);
      }
#pragma unroll
      for (int h = 0; h < 2; ++h) {
        const f32x4 cq = *(const f32x4*)(csl + h * 16 + 4 * g);
        const f32x4 qq = (aq[h] - mean * cq) * rstd;
        const f32x4 pq = mfma16(wph[h], pad8(cvt4(qq)), zf);
        bf16x4 pl, mi;
        dual_softmax(pq, pl, mi);
        phq[h] = cat8(pl, mi);
      }
      SB
      {
        wait_vm<8>(pt[0]);
        f32x4 s = __builtin_bit_cast(f32x4, pt[0]);
#define KVS(i) wait_vm<8 - (i)>(pt[i]); s += __builtin_bit_cast(f32x4, pt[i]);
        KVS(1) KVS(2) KVS(3) KVS(4) KVS(5) KVS(6) KVS(7)
#undef KVS
#pragma unroll
        for (int j = 0; j < 4; ++j) s[j] = half_add(s[j]);
        char* kvb = smem + SCR;
        if (l < 32) *(bf16x4*)(kvb + (rt >> 1) * 1024 + (kh * 32 + l) * 16 + (rt & 1) * 8) = cvt4(s);
        wait_vm<0>(pz);
        if (kh == 0) {
          f32x4 z = __builtin_bit_cast(f32x4, pz);
#pragma unroll
          for (int j = 0; j < 4; ++j) z[j] = ror_add<1>(ror_add<2>(ror_add<4>(ror_add<8>(z[j]))));
          if (r == 0) *(bf16x4*)(kvb + 2048 + (rt >> 1) * 64 + g * 16 + (rt & 1) * 8) = cvt4(z);
        }
      }
    }
    ST(7)
    bar();
    u32x4 xres;
    {
      LANE_VARS
      const char* kvb = smem + SCR;
      bf16x4 ov[2];
#pragma unroll
      for (int h = 0; h < 2; ++h) {
        const bf16x8 kvA = *(const bf16x8*)(kvb + h * 1024 + l * 16);
        const bf16x8 zA = *(const bf16x8*)(kvb + 2048 + h * 64 + g * 16);
        const f32x4 num = mfma16(kvA, phq[h], zf);
        const f32x4 den = mfma16(zA, phq[h], zf);
        const float dn = bfr(bfr(den[0]) + 1e-6f);
        f32x4 v;
#pragma unroll
        for (int j = 0; j < 4; ++j) v[j] = bfr(num[j]) / dn;
        ov[h] = cvt4(v);
      }
      bstore(asu(cat8(ov[0], ov[1])), l16, rst, lbase + O_O + (lrow * 16 + hp) * 1024);
      bload<0>(xres, l16, rs, xin + sblk);
      wait_vm<0>(xres);
    }
    ST(8)
    bar();
    if (w == 0) {
      unsigned pv;
      const int l = opq(l0);
      const unsigned po = poll_off(fa + 1, (mt >> 1) * 16, 16, l);
      if (l == 0) flag_store(ws, (unsigned)((fa + 1) * 1024 + (rb * 16 + hp) * 4));
      poll_issue(pv, ws, po);
      poll_finish(pv, ws, po);
    }
    const unsigned myflag = (unsigned)(mt * 8 + nt) * 4;
    bf16x8 x1;
    ST(9)
    {
      LANE_VARS
      bar();
      u32x4 a[8];
      const unsigned so = lbase + O_O + srow * 16384 + kh * 8192;
#define LDA(i) bload<((i) & 3) * 1024>(a[i], l16, rs, so + ((i) >> 2) * 4096);
      LDA(0) LDA(1) LDA(2) LDA(3) LDA(4) LDA(5) LDA(6) LDA(7)
      f32x4 acc[4];
      float d0, d1;
      s_loop<96, 128, 3, 0, false>(smem, woffS, kh, a, acc, d0, d1);
      *(f32x4*)(xs + l16) = acc[2];
      *(f32x4*)(xs + 1024 + l16) = acc[3];
      ST(10)
      bar();
      acc[0] += *(const f32x4*)(xp + l16);
      acc[1] += *(const f32x4*)(xp + 1024 + l16);
      const bf16x8 xr = opq8(asb(xres));
      x1 = cat8(cvt4(up4(lo4(xr)) + bfr4(acc[0])), cvt4(up4(hi4(xr)) + bfr4(acc[1])));
      bstore(asu(x1), l16, rst, lbase + O_X1 + sblk);
      wait_vm<0>();
    }
    ST(11)
    bar();
    if (w == 0) {
      unsigned pv;
      const int l = opq(l0);
      const unsigned po = poll_off(fa + 2, mt * 8, 8, l);
      if (l == 0) flag_store(ws, (unsigned)(fa + 2) * 1024 + myflag);
      poll_issue(pv, ws, po);
      poll_finish(pv, ws, po);
    }
    bf16x8 xn;
    ST(12)
    {
      LANE_VARS
      bar();
      u32x4 a[8];
      const unsigned so = lbase + O_X1 + srow * 16384 + kh * 8192;
      LDA(0) LDA(1) LDA(2) LDA(3) LDA(4) LDA(5) LDA(6) LDA(7)
      f32x4 acc[4];
      float ssum = 0.f, ssq = 0.f;
      s_loop<20, 52, 8, 0, true>(smem, woffS, kh, a, acc, ssum, ssq);
      *(f32x4*)(xs + l16) = acc[2];
      *(f32x4*)(xs + 1024 + l16) = acc[3];
      ssum = xg_add(ssum); ssq = xg_add(ssq);
      if (g == 0) *(f32x2*)(xs + 2048 + r * 8) = (f32x2){ssum, ssq};
      ST(13)
      bar();
      acc[0] += *(const f32x4*)(xp + l16);
      acc[1] += *(const f32x4*)(xp + 1024 + l16);
      const f32x2 sp = *(const f32x2*)(xp + 2048 + r * 8);
      const float mean = (ssum + sp[0]) * (1.0f / DM);
      float var = (ssq + sp[1]) * (1.0f / DM) - mean * mean;
      var = var < 0.f ? 0.f : var;
      const float rstd = __builtin_amdgcn_rsqf(var + 1e-5f);
      const float scale = rstd * __builtin_amdgcn_rsqf(var * rstd * rstd + 1e-5f);
      bf16x4 hv[2];
#pragma unroll
      for (int t = 0; t < 2; ++t) {
        const f32x4 c4 = *(const f32x4*)(csl + 96 + (2 * kh + t) * 16 + 4 * g);
        f32x4 v = bfr4((acc[t] - mean * c4) * scale);
#pragma unroll
        for (int j = 0; j < 4; ++j) v[j] = gelu_t(v[j]);
        hv[t] = cvt4(v);
      }
      bstore(asu(cat8(hv[0], hv[1])), l16, rst, lbase + O_H + sblk);
      x1 = opq8(x1);
      xn = cat8(cvt4((up4(lo4(x1)) - mean) * rstd), cvt4((up4(hi4(x1)) - mean) * rstd));
      wait_vm<0>();
    }
    ST(14)
    bar();
    if (w == 0) {
      unsigned pv;
      const int l = opq(l0);
      const unsigned po = poll_off(fa + 3, mt * 8 - 8, 24, l);
      if (l == 0) flag_store(ws, (unsigned)(fa + 3) * 1024 + myflag);
      poll_issue(pv, ws, po);
      poll_finish(pv, ws, po);
    }
    bf16x8 y1;
    ST(15)
    {
      LANE_VARS
      bar();
      u32x4 a[8];
      const u32x4 rh = mk_rsrc(ws + lbase + O_H, 2u << 20);
      const int s0 = (int)srow * 16 + r - (1 + kh), s1 = (int)srow * 16 + r + (1 + kh);
      const unsigned v0 = (unsigned)((s0 >> 4) * 16384 + kh * 8192 + ((s0 & 15) + 16 * g) * 16);
      const unsigned v1 = (unsigned)((s1 >> 4) * 16384 + kh * 8192 + 4096 + ((s1 & 15) + 16 * g) * 16);
      const unsigned z0 = 0u;
      bload<0>(a[0], v0, rh, z0); bload<1024>(a[1], v0, rh, z0); bload<2048>(a[2], v0, rh, z0); bload<3072>(a[3], v0, rh, z0);
      bload<0>(a[4], v1, rh, z0); bload<1024>(a[5], v1, rh, z0); bload<2048>(a[6], v1, rh, z0); bload<3072>(a[7], v1, rh, z0);
      f32x4 acc[4];
      float d0, d1;
      s_loop<84, 116, 6, 0, false>(smem, woffS, kh, a, acc, d0, d1);
      *(f32x4*)(xs + l16) = acc[2];
      *(f32x4*)(xs + 1024 + l16) = acc[3];
      ST(16)
      bar();
      acc[0] += *(const f32x4*)(xp + l16);
      acc[1] += *(const f32x4*)(xp + 1024 + l16);
      xn = opq8(xn);
      y1 = cat8(cvt4(up4(lo4(xn)) + bfr4(acc[0])), cvt4(up4(hi4(xn)) + bfr4(acc[1])));
      bstore(asu(y1), l16, rst, lbase + O_Y1 + sblk);
      wait_vm<0>();
    }
    ST(17)
    bar();
    if (w == 0) {
      unsigned pv;
      const int l = opq(l0);
      const unsigned po = poll_off(fa + 4, rb * 16, 16, l);
      if (l == 0) flag_store(ws, (unsigned)(fa + 4) * 1024 + myflag);
      poll_issue(pv, ws, po);
      poll_finish(pv, ws, po);
    }
    ST(18)
    {
      LANE_VARS
      bar();
      u32x4 a[16];
      const unsigned so = lbase + O_Y1 + lrow * 16384;
      LDA(0) LDA(1) LDA(2) LDA(3) LDA(4) LDA(5) LDA(6) LDA(7)
      LDA(8) LDA(9) LDA(10) LDA(11) LDA(12) LDA(13) LDA(14) LDA(15)
      bf16x8 wf[16][6];
      f32x4 acc[6];
      float ssum = 0.f, ssq = 0.f;
      const int e0 = w96E + 8192, od0 = w96O + 8192, e1 = e0 + 49152, od1 = od0 + 49152;
#define P5_RD(i)                                                                                   \
  _Pragma("unroll") for (int t = 0; t < 6; ++t) {                                                  \
    const int o_ = ((i) < 8 ? ((t & 1) ? od0 : e0) : ((t & 1) ? od1 : e1)) + ((i) & 7) * 6144 + t * 32; \
    wf[i][t] = tr_frag(smem, o_, o_ + 3072);                                                       \
  }
      P5_RD(0) P5_RD(1)
#pragma unroll
      for (int t = 0; t < 6; ++t) acc[t] = zf;
#define P5_STEP(i)                                                                        \
  {                                                                                       \
    wait_vm<15 - (i)>(a[i]);                                                              \
    const bf16x8 af = asb(a[i]);                                                          \
    _Pragma("unroll") for (int t = 0; t < 6; ++t) acc[t] = mfma16(wf[i][t], af, acc[t]);  \
    ssum = dot8(af, ones, ssum); ssq = dot8(af, af, ssq);                                 \
    if constexpr ((i) == 6) { SB bar(); }                                      \
    if constexpr ((i) + 2 < 16) { P5_RD(((i) + 2) & 15) }                                 \
    if constexpr ((i) == 14) { SB ST(19) bar(); }                              \
    SB                                                                                    \
  }
      P5_STEP(0) P5_STEP(1) P5_STEP(2) P5_STEP(3) P5_STEP(4) P5_STEP(5) P5_STEP(6) P5_STEP(7)
      P5_STEP(8) P5_STEP(9) P5_STEP(10) P5_STEP(11) P5_STEP(12) P5_STEP(13) P5_STEP(14) P5_STEP(15)
#undef P5_STEP
#undef P5_RD
      ssum = xg_add(ssum); ssq = xg_add(ssq);
      const float mean = ssum * (1.0f / DM);
      float var = ssq * (1.0f / DM) - mean * mean;
      var = var < 0.f ? 0.f : var;
      const float rstd = __builtin_amdgcn_rsqf(var + 1e-5f);
#pragma unroll
      for (int c = 0; c < 3; ++c) {
        if (hp * 3 + c < 43) {
          bf16x4 hv[2];
#pragma unroll
          for (int t = 0; t < 2; ++t) {
            const f32x4 c4 = *(const f32x4*)(csl + 160 + (2 * c + t) * 16 + 4 * g);
            f32x4 v = bfr4((acc[2 * c + t] - mean * c4) * rstd);
#pragma unroll
            for (int j = 0; j < 4; ++j) v[j] = gelu_t(v[j]);
            hv[t] = cvt4(v);
          }
          bstore(asu(cat8(hv[0], hv[1])), l16, rst, lbase + O_H2 + (lrow * 43 + hp * 3 + c) * 1024);
        }
      }
      wait_vm<0>();
    }
    ST(20)
    bar();
    if (w == 0) {
      unsigned pv;
      const int l = opq(l0);
      const unsigned po = poll_off(fa + 5, (mt >> 1) * 16, 16, l);
      if (l == 0) flag_store(ws, (unsigned)((fa + 5) * 1024 + (rb * 16 + hp) * 4));
      poll_issue(pv, ws, po);
      poll_finish(pv, ws, po);
    }
    ST(21)
    {
      LANE_VARS
      bar();
      u32x4 a[22];
      const unsigned so = lbase + O_H2 + (srow * 43 + kh * 22) * 1024;
      const unsigned so_last = lbase + O_H2 + (srow * 43 + 21 + kh * 21) * 1024;
#define LD6(i) { if constexpr ((i) == 21) bload<0>(a[21], l16, rs, so_last); else bload<((i) & 3) * 1024>(a[i], l16, rs, so + ((i) >> 2) * 4096); }
      LD6(0) LD6(1) LD6(2) LD6(3) LD6(4) LD6(5) LD6(6) LD6(7) LD6(8) LD6(9) LD6(10) LD6(11)
      int o3[4], o2[4];
#pragma unroll
      for (int t = 0; t < 4; ++t) {
        o3[t] = (woffS + (4 + 4 * kh) * 1024) ^ (t << 5);
        o2[t] = (woffS + (kh ? 0 : 136) * 1024) ^ (t << 5);
      }
      bf16x8 wf[22][4];
      f32x4 acc[4];
#define P6_RD(i)                                                                                    \
  _Pragma("unroll") for (int t = 0; t < 4; ++t) {                                                   \
    const int o_ = (i) < 4 ? o3[t] + (100 + 8 * (i)) * 1024 : ((i) == 4 ? o2[t] : o3[t] + ((i) - 5) * 8192); \
    wf[i][t] = tr_frag(smem, o_, o_ + 2048);                                                        \
  }
      P6_RD(0) P6_RD(1) P6_RD(2) P6_RD(3)
      bar();
#pragma unroll
      for (int t = 0; t < 4; ++t) acc[t] = zf;
#define P6_STEP(i)                                                                        \
  {                                                                                       \
    wait_vm<((i) <= 10 ? 11 : 21 - (i))>(a[i]);                                           \
    const bf16x8 af = asb(a[i]);                                                          \
    _Pragma("unroll") for (int t = 0; t < 4; ++t) acc[t] = mfma16(wf[i][t], af, acc[t]);  \
    if constexpr ((i) + 12 < 22) LD6(((i) + 12) % 22)                                     \
    if constexpr ((i) == 13) { SB bar(); }                                     \
    if constexpr ((i) + 4 < 21) { P6_RD(((i) + 4) % 22) }                                 \
    SB                                                                                    \
  }
      P6_STEP(0) P6_STEP(1) P6_STEP(2) P6_STEP(3) P6_STEP(4) P6_STEP(5) P6_STEP(6) P6_STEP(7)
      P6_STEP(8) P6_STEP(9) P6_STEP(10) P6_STEP(11) P6_STEP(12) P6_STEP(13) P6_STEP(14) P6_STEP(15)
      P6_STEP(16) P6_STEP(17) P6_STEP(18) P6_STEP(19) P6_STEP(20)
      wait_vm<0>(a[21]);
      if (kh == 0) {
        P6_RD(21)
        const bf16x8 af = asb(a[21]);
#pragma unroll
        for (int t = 0; t < 4; ++t) acc[t] = mfma16(wf[21][t], af, acc[t]);
      }
#undef P6_STEP
#undef P6_RD
#undef LD6
#undef LDA
      *(f32x4*)(xs + l16) = acc[2];
      *(f32x4*)(xs + 1024 + l16) = acc[3];
      ST(22)
      bar();
      acc[0] += *(const f32x4*)(xp + l16);
      acc[1] += *(const f32x4*)(xp + 1024 + l16);
      x1 = opq8(x1); y1 = opq8(y1);
      const bf16x4 r0 = cvt4(up4(lo4(x1)) + bfr4(up4(lo4(y1)) + bfr4(acc[0])));
      const bf16x4 r1 = cvt4(up4(hi4(x1)) + bfr4(up4(hi4(y1)) + bfr4(acc[1])));
      if (layer == 0) {
        bstore(asu(cat8(r0, r1)), l16, rst, lbase + O_X2 + sblk);
        wait_vm<0>();
      } else {
        u16* op = out + (size_t)(srow * 16 + r) * DM + (nt * 2 + kh) * 32 + 4 * g;
        *(bf16x4*)op = r0;
        *(bf16x4*)(op + 16) = r1;
      }
    }
    ST(23)
    bar();
    if (layer == 0 && w == 0) {
      unsigned pv;
      const int l = opq(l0);
      const unsigned po = poll_off(6, rb * 16, 16, l);
      if (l == 0) flag_store(ws, 6u * 1024 + myflag);
      poll_issue(pv, ws, po);
      poll_finish(pv, ws, po);
    }
  }
}

extern "C" void kernel_launch(void* const* d_in, const int* in_sizes, int n_in, void* d_out, int out_size,
                              void* d_ws, size_t ws_size, hipStream_t stream) {
  const u16* x0 = (const u16*)d_in[0];
  const u16* wqkv = (const u16*)d_in[1];
  const u16* wphi = (const u16*)d_in[2];
  const u16* wo = (const u16*)d_in[3];
  const u16* w1 = (const u16*)d_in[5];
  const u16* w2 = (const u16*)d_in[6];
  const u16* w3 = (const u16*)d_in[7];
  const u16* w4 = (const u16*)d_in[8];
  char* ws = (char*)d_ws;
  setup_k<<<256, 256, 0, stream>>>(x0, wqkv, w1, w3, ws);
  mega_k<<<256, 640, 0, stream>>>(wqkv, wphi, wo, w1, w2, w3, w4, ws, (u16*)d_out);
}
```

```cpp
#include <hip/hip_runtime.h>
#include <stdint.h>

typedef __attribute__((ext_vector_type(8))) __bf16 bf16x8;
typedef __attribute__((ext_vector_type(4))) __bf16 bf16x4;
typedef __attribute__((ext_vector_type(4))) float f32x4;
typedef __attribute__((ext_vector_type(2))) float f32x2;
typedef __attribute__((ext_vector_type(4))) unsigned u32x4;
typedef __attribute__((ext_vector_type(2))) unsigned u32x2;
typedef unsigned short u16;

#define LDSP(T, p) ((__attribute__((address_space(3))) T*)(p))
#define SB __builtin_amdgcn_sched_barrier(0);

#define ST(i)
#define CNT(i)

static constexpr int DM = 512;
static constexpr int FF = 1376;
static constexpr unsigned WS_CS = 64u << 10;
static constexpr unsigned WS_XT = 1u << 20;
static constexpr unsigned WS_L0 = 4u << 20, WS_LSTR = 20u << 20;
static constexpr unsigned O_KVR = 0, O_JUNK = 1u << 20, O_O = 2u << 20, O_X1 = 4u << 20, O_H = 6u << 20,
                          O_Y1 = 8u << 20, O_X2 = 10u << 20, O_H2 = 12u << 20, O_KVP = 35u << 19;
static constexpr unsigned KVREC = 2304;
static constexpr unsigned KVR = 1152;
static constexpr int SLOT1 = 65536;
static constexpr int L_E = 131072;
static constexpr int L_S = L_E + 16384;
static constexpr int L_CS = L_S + 1024;
static constexpr int L_PHI = L_CS + 1792;
static constexpr int L_KV = L_PHI + 2048;
static constexpr int L_ST = L_KV + 4 * 1088;
static constexpr int T_PHI = 0, T_V = 64 * 288, T_E = 32768;
#define SPINCAP 400000u
static constexpr unsigned SPIN_CAP = SPINCAP;
#define DL0 2
#define DLK 2
#define DL3 2
#define DL4 2
#define DL5 2
#define DL6 2

__device__ __forceinline__ f32x4 mfma16(bf16x8 a, bf16x8 b, f32x4 c) {
  return __builtin_amdgcn_mfma_f32_16x16x32_bf16(a, b, c, 0, 0, 0);
}
__device__ __forceinline__ bf16x4 tr4(const char* smem, int off) {
  return __builtin_amdgcn_ds_read_tr16_b64_v4bf16(LDSP(bf16x4, smem + off));
}
__device__ __forceinline__ bf16x8 tr_frag(const char* smem, int off0, int off1) {
  bf16x4 v0 = tr4(smem, off0);
  bf16x4 v1 = tr4(smem, off1);
  return __builtin_shufflevector(v0, v1, 0, 1, 2, 3, 4, 5, 6, 7);
}
__device__ __forceinline__ void glds16(const void* base, unsigned voff, unsigned lds) {
  unsigned keep;
  asm volatile("s_mov_b32 %0, m0\n\ts_mov_b32 m0, %3\n\ts_nop 0\n\tglobal_load_lds_dwordx4 %1, %2\n\ts_mov_b32 m0, %0"
               : "=&s"(keep) : "v"(voff), "s"(base), "s"(lds) : "memory");
}
__device__ __forceinline__ unsigned lds_addr(const char* p) { return (unsigned)(uintptr_t)LDSP(const char, p); }
template <int N>
__device__ __forceinline__ void wait_vm() {
  asm volatile("s_waitcnt vmcnt(%0)" ::"n"(N) : "memory");
}
template <int N>
__device__ __forceinline__ void wait_vm(u32x4& a) {
  asm volatile("s_waitcnt vmcnt(%1)" : "+v"(a) : "n"(N) : "memory");
}
__device__ __forceinline__ void tie(u32x4& a) { asm volatile("" : "+v"(a)::"memory"); }
__device__ __forceinline__ void bar0() {
  asm volatile("" ::: "memory");
  __builtin_amdgcn_s_barrier();
  asm volatile("" ::: "memory");
}
__device__ __forceinline__ void bar() {
  asm volatile("s_waitcnt lgkmcnt(0)" ::: "memory");
  __builtin_amdgcn_s_barrier();
  asm volatile("" ::: "memory");
}
template <int N>
__device__ __forceinline__ void nap() {
  if constexpr (N > 0) asm volatile("s_sleep %0" ::"n"(N) : "memory");
}
template <int IMM>
__device__ __forceinline__ void bload(u32x4& d, unsigned voff, u32x4 rs, unsigned soff) {
  asm volatile("buffer_load_dwordx4 %0, %1, %2, %3 offen offset:%4 sc1"
               : "=v"(d) : "v"(voff), "s"(rs), "s"(soff), "n"(IMM) : "memory");
}
__device__ __forceinline__ void bstore(u32x4 d, unsigned voff, __amdgpu_buffer_rsrc_t rs, unsigned soff) {
  __builtin_amdgcn_raw_buffer_store_b128(d, rs, voff, soff, 16);
  asm volatile("s_nop 1" ::"v"(d) : "memory");
}
__device__ __forceinline__ unsigned max4(u32x4 a) {
  unsigned m = a[0] > a[1] ? a[0] : a[1];
  m = m > a[2] ? m : a[2];
  return m > a[3] ? m : a[3];
}
__device__ __forceinline__ bool poisoned(u32x4 a) { return __any(max4(a) == 0xffffffffu); }
template <int IMM, int ID>
__device__ __forceinline__ void fixup(u32x4& a, unsigned voff, u32x4 rs, unsigned soff) {
  if (__builtin_expect(poisoned(a), 0)) {
    unsigned n = 0;
    CNT(ID)
    do {
      CNT(ID + 16)
      nap<1>();
      bload<IMM>(a, voff, rs, soff);
      wait_vm<0>(a);
    } while (poisoned(a) && ++n < SPIN_CAP);
  }
}
__device__ __forceinline__ u32x4 mk_rsrc(const void* p, unsigned bytes) {
  const unsigned long long a = (unsigned long long)p;
  u32x4 r;
  r[0] = (unsigned)a; r[1] = (unsigned)(a >> 32) & 0xffffu; r[2] = bytes; r[3] = 0x00020000u;
  return r;
}
__device__ __forceinline__ bf16x8 asb(u32x4 v) { return __builtin_bit_cast(bf16x8, v); }
__device__ __forceinline__ u32x4 asu(bf16x8 v) { return __builtin_bit_cast(u32x4, v); }
__device__ __forceinline__ f32x4 asf(u32x4 v) { return __builtin_bit_cast(f32x4, v); }
__device__ __forceinline__ u32x4 fsu(f32x4 v) { return __builtin_bit_cast(u32x4, v); }
__device__ __forceinline__ float dot8(bf16x8 a, bf16x8 b, float c) {
  c = __builtin_amdgcn_fdot2_f32_bf16(__builtin_shufflevector(a, a, 0, 1), __builtin_shufflevector(b, b, 0, 1), c, false);
  c = __builtin_amdgcn_fdot2_f32_bf16(__builtin_shufflevector(a, a, 2, 3), __builtin_shufflevector(b, b, 2, 3), c, false);
  c = __builtin_amdgcn_fdot2_f32_bf16(__builtin_shufflevector(a, a, 4, 5), __builtin_shufflevector(b, b, 4, 5), c, false);
  c = __builtin_amdgcn_fdot2_f32_bf16(__builtin_shufflevector(a, a, 6, 7), __builtin_shufflevector(b, b, 6, 7), c, false);
  return c;
}
__device__ __forceinline__ float bfr(float x) { return (float)(__bf16)x; }
__device__ __forceinline__ bf16x4 cvt4(f32x4 v) {
  bf16x4 r;
  r[0] = (__bf16)v[0]; r[1] = (__bf16)v[1]; r[2] = (__bf16)v[2]; r[3] = (__bf16)v[3];
  return r;
}
__device__ __forceinline__ f32x4 up4(bf16x4 v) {
  f32x4 r;
  r[0] = (float)v[0]; r[1] = (float)v[1]; r[2] = (float)v[2]; r[3] = (float)v[3];
  return r;
}
typedef __attribute__((ext_vector_type(2))) __bf16 bf16x2;
__device__ __forceinline__ unsigned pk2(float a, float b) {
  const f32x2 v = {a, b};
  return __builtin_bit_cast(unsigned, __builtin_convertvector(v, bf16x2));
}
__device__ __forceinline__ float lo_f(unsigned u) { return __builtin_bit_cast(float, u << 16); }
__device__ __forceinline__ float hi_f(unsigned u) { return __builtin_bit_cast(float, u & 0xffff0000u); }
__device__ __forceinline__ f32x4 bfr4(f32x4 v) {
  const unsigned a = pk2(v[0], v[1]), b = pk2(v[2], v[3]);
  return (f32x4){lo_f(a), hi_f(a), lo_f(b), hi_f(b)};
}
__device__ __forceinline__ bf16x4 lo4(bf16x8 v) { return __builtin_shufflevector(v, v, 0, 1, 2, 3); }
__device__ __forceinline__ bf16x4 hi4(bf16x8 v) { return __builtin_shufflevector(v, v, 4, 5, 6, 7); }
__device__ __forceinline__ bf16x8 cat8(bf16x4 a, bf16x4 b) { return __builtin_shufflevector(a, b, 0, 1, 2, 3, 4, 5, 6, 7); }
__device__ __forceinline__ bf16x8 pad8(bf16x4 v) {
  bf16x4 z = {(__bf16)0.f, (__bf16)0.f, (__bf16)0.f, (__bf16)0.f};
  return cat8(v, z);
}
__device__ __forceinline__ bf16x8 ones8() {
  bf16x8 r;
#pragma unroll
  for (int i = 0; i < 8; ++i) r[i] = (__bf16)1.0f;
  return r;
}
__device__ __forceinline__ float xg_add(float v) {
  unsigned x = __builtin_bit_cast(unsigned, v);
  auto r = __builtin_amdgcn_permlane16_swap(x, x, false, false);
  v = __builtin_bit_cast(float, (unsigned)r[0]) + __builtin_bit_cast(float, (unsigned)r[1]);
  x = __builtin_bit_cast(unsigned, v);
  auto r2 = __builtin_amdgcn_permlane32_swap(x, x, false, false);
  return __builtin_bit_cast(float, (unsigned)r2[0]) + __builtin_bit_cast(float, (unsigned)r2[1]);
}
__device__ __forceinline__ float xg_max(float v) {
  unsigned x = __builtin_bit_cast(unsigned, v);
  auto r = __builtin_amdgcn_permlane16_swap(x, x, false, false);
  v = fmaxf(__builtin_bit_cast(float, (unsigned)r[0]), __builtin_bit_cast(float, (unsigned)r[1]));
  x = __builtin_bit_cast(unsigned, v);
  auto r2 = __builtin_amdgcn_permlane32_swap(x, x, false, false);
  return fmaxf(__builtin_bit_cast(float, (unsigned)r2[0]), __builtin_bit_cast(float, (unsigned)r2[1]));
}
template <int N>
__device__ __forceinline__ float ror_add(float v) {
  int x = __builtin_bit_cast(int, v);
  int y = __builtin_amdgcn_update_dpp(x, x, 0x120 + N, 0xf, 0xf, false);
  return v + __builtin_bit_cast(float, y);
}
__device__ __forceinline__ int opq(int v) { asm volatile("" : "+v"(v)); return v; }
__device__ __forceinline__ int opqs(int v) { asm volatile("" : "+s"(v)); return v; }
__device__ __forceinline__ bf16x8 opq8(bf16x8 v) { u32x4 u = asu(v); asm volatile("" : "+v"(u)); return asb(u); }

template <int V> struct IC { static constexpr int value = V; };
template <int I, int N, class F>
__device__ __forceinline__ void sfor(F&& f) {
  if constexpr (I < N) { f(IC<I>{}); sfor<I + 1, N>(f); }
}

template <class Hook>
__device__ __forceinline__ u32x4 gelu8(f32x4 a, f32x4 b, Hook hk) {
  const float c1 = -2.0f * 0.7978845608028654f * 1.4426950408889634f;
  const float c2 = c1 * 0.044715f;
  a = bfr4(a); b = bfr4(b);
  SB hk(IC<1>{}); SB
  f32x4 ta = a * a, tb = b * b;
  ta = ta * c2 + c1; tb = tb * c2 + c1;
  SB hk(IC<2>{}); SB
  ta *= a; tb *= b;
  SB
#pragma unroll
  for (int j = 0; j < 4; ++j) ta[j] = __builtin_amdgcn_exp2f(ta[j]);
  SB hk(IC<3>{}); SB
#pragma unroll
  for (int j = 0; j < 4; ++j) tb[j] = __builtin_amdgcn_exp2f(tb[j]);
  SB hk(IC<4>{}); SB
  ta += 1.0f; tb += 1.0f;
  SB
#pragma unroll
  for (int j = 0; j < 4; ++j) ta[j] = __builtin_amdgcn_rcpf(ta[j]);
  SB hk(IC<5>{}); SB
#pragma unroll
  for (int j = 0; j < 4; ++j) tb[j] = __builtin_amdgcn_rcpf(tb[j]);
  SB hk(IC<6>{}); SB
  ta *= a; tb *= b;
  SB
  const u32x4 r = {pk2(ta[0], ta[1]), pk2(ta[2], ta[3]), pk2(tb[0], tb[1]), pk2(tb[2], tb[3])};
  return r;
}
template <class Hook>
__device__ __forceinline__ void dual_softmax2(f32x4 p0, f32x4 p1, u32x4& o0, u32x4& o1, Hook hk) {
  const float L2E = 1.4426950408889634f;
  p0 = bfr4(p0) * L2E; p1 = bfr4(p1) * L2E;
  float m[4];
  m[0] = fmaxf(fmaxf(p0[0], p0[1]), fmaxf(p0[2], p0[3]));
  m[1] = -fminf(fminf(p0[0], p0[1]), fminf(p0[2], p0[3]));
  m[2] = fmaxf(fmaxf(p1[0], p1[1]), fmaxf(p1[2], p1[3]));
  m[3] = -fminf(fminf(p1[0], p1[1]), fminf(p1[2], p1[3]));
  SB hk(IC<1>{}); SB
#pragma unroll
  for (int i = 0; i < 4; ++i) {
    const unsigned x = __builtin_bit_cast(unsigned, m[i]);
    auto r = __builtin_amdgcn_permlane16_swap(x, x, false, false);
    m[i] = fmaxf(__builtin_bit_cast(float, (unsigned)r[0]), __builtin_bit_cast(float, (unsigned)r[1]));
  }
  SB
#pragma unroll
  for (int i = 0; i < 4; ++i) {
    const unsigned x = __builtin_bit_cast(unsigned, m[i]);
    auto r = __builtin_amdgcn_permlane32_swap(x, x, false, false);
    m[i] = fmaxf(__builtin_bit_cast(float, (unsigned)r[0]), __builtin_bit_cast(float, (unsigned)r[1]));
  }
  SB hk(IC<2>{}); SB
  f32x4 e[4];
  e[0] = p0 - m[0]; e[1] = -p0 - m[1]; e[2] = p1 - m[2]; e[3] = -p1 - m[3];
  SB
#pragma unroll
  for (int j = 0; j < 4; ++j) { e[0][j] = __builtin_amdgcn_exp2f(e[0][j]); e[1][j] = __builtin_amdgcn_exp2f(e[1][j]); }
  SB hk(IC<3>{}); SB
#pragma unroll
  for (int j = 0; j < 4; ++j) { e[2][j] = __builtin_amdgcn_exp2f(e[2][j]); e[3][j] = __builtin_amdgcn_exp2f(e[3][j]); }
  SB hk(IC<4>{}); SB
#pragma unroll
  for (int i = 0; i < 4; ++i) m[i] = (e[i][0] + e[i][1]) + (e[i][2] + e[i][3]);
  SB
#pragma unroll
  for (int i = 0; i < 4; ++i) {
    const unsigned x = __builtin_bit_cast(unsigned, m[i]);
    auto r = __builtin_amdgcn_permlane16_swap(x, x, false, false);
    m[i] = __builtin_bit_cast(float, (unsigned)r[0]) + __builtin_bit_cast(float, (unsigned)r[1]);
  }
  SB hk(IC<5>{}); SB
#pragma unroll
  for (int i = 0; i < 4; ++i) {
    const unsigned x = __builtin_bit_cast(unsigned, m[i]);
    auto r = __builtin_amdgcn_permlane32_swap(x, x, false, false);
    m[i] = __builtin_bit_cast(float, (unsigned)r[0]) + __builtin_bit_cast(float, (unsigned)r[1]);
  }
  SB
#pragma unroll
  for (int i = 0; i < 4; ++i) m[i] = __builtin_amdgcn_rcpf(m[i]);
  SB hk(IC<6>{}); SB
#pragma unroll
  for (int i = 0; i < 4; ++i) e[i] *= m[i];
  SB
  o0 = (u32x4){pk2(e[0][0], e[0][1]), pk2(e[0][2], e[0][3]), pk2(e[1][0], e[1][1]), pk2(e[1][2], e[1][3])};
  o1 = (u32x4){pk2(e[2][0], e[2][1]), pk2(e[2][2], e[2][3]), pk2(e[3][0], e[3][1]), pk2(e[3][2], e[3][3])};
}

__global__ __launch_bounds__(256) void setup_k(const u16* __restrict__ x0, const u16* __restrict__ wqkv,
                                               const u16* __restrict__ w1, const u16* __restrict__ w3,
                                               char* __restrict__ ws) {
  __shared__ float red[4][64];
  const int b = blockIdx.x, tid = threadIdx.x;
  u32x4 v[4];
  const int cb = b >> 2, qr = b & 3, c = tid & 7;
  if (b < 216) {
    const u16* W; int ldw, c0;
    if (cb < 24) { W = wqkv; ldw = 1536; c0 = cb * 64; }
    else if (cb < 32) { W = w1; ldw = 512; c0 = (cb - 24) * 64; }
    else { W = w3; ldw = FF; c0 = (cb - 32) * 64; }
    int col = c0 + c * 8;
    col = col < ldw ? col : c0;
    const u16* p = W + (size_t)(qr * 128 + (tid >> 3)) * ldw + col;
#pragma unroll
    for (int i = 0; i < 4; ++i) v[i] = *(const u32x4*)(p + (size_t)i * 32 * ldw);
  }
#pragma unroll
  for (int it = 0; it < 2; ++it) {
    const int o = (it * 256 + b) * 256 + tid;
    const int l = o & 63, blk = o >> 6, cg = blk & 15, T = blk >> 4, r = l & 15, g = l >> 4;
    const u16* src = x0 + (size_t)(T * 16 + r) * DM + cg * 32 + 4 * g;
    const u32x2 lo = *(const u32x2*)src, hi = *(const u32x2*)(src + 16);
    *(u32x4*)(ws + WS_XT + (size_t)o * 16) = (u32x4){lo[0], lo[1], hi[0], hi[1]};
  }
  {
    const u32x4 pz = {0xffffffffu, 0xffffffffu, 0xffffffffu, 0xffffffffu};
    const unsigned t16 = (unsigned)(b * 256 + tid) * 16;
#pragma unroll
    for (int L = 0; L < 2; ++L) {
      char* base = ws + WS_L0 + L * WS_LSTR;
      *(u32x4*)(base + O_O + t16) = pz;
      *(u32x4*)(base + O_O + (1u << 20) + t16) = pz;
      *(u32x4*)(base + O_KVP + t16) = pz;
      *(u32x4*)(base + O_KVP + (1u << 20) + t16) = pz;
      if (t16 < 1024 * KVREC - (2u << 20)) *(u32x4*)(base + O_KVP + (2u << 20) + t16) = pz;
      if (t16 < 32 * KVR) *(u32x4*)(base + O_KVR + t16) = pz;
    }
  }
  if (b < 216) {
    float s[8];
#pragma unroll
    for (int j = 0; j < 8; ++j) {
      const int sh = (j & 1) ? 0 : 16;
      const unsigned mk = 0xffff0000u;
      s[j] = (__builtin_bit_cast(float, (v[0][j >> 1] << sh) & mk) + __builtin_bit_cast(float, (v[1][j >> 1] << sh) & mk)) +
             (__builtin_bit_cast(float, (v[2][j >> 1] << sh) & mk) + __builtin_bit_cast(float, (v[3][j >> 1] << sh) & mk));
    }
#pragma unroll
    for (int m = 8; m < 64; m <<= 1)
#pragma unroll
      for (int j = 0; j < 8; ++j) s[j] += __shfl_xor(s[j], m);
    if ((tid & 63) < 8) {
#pragma unroll
      for (int j = 0; j < 8; ++j) red[tid >> 6][c * 8 + j] = s[j];
    }
    __syncthreads();
    if (tid < 64)
      ((float*)(ws + WS_CS))[qr * 3456 + cb * 64 + tid] = (red[0][tid] + red[1][tid]) + (red[2][tid] + red[3][tid]);
  }
}

template <int NK, int ROLLN, bool STATS, bool SKIPLAST, bool LAG, class WaitF, class ExtraF>
__device__ __forceinline__ void unit_loop(bf16x8 (&B)[8][4], u32x4 (&a)[8], f32x4 (&acc)[4], const char* smem,
                                          int rbase, float& ssum, float& ssq, bool skip, WaitF wf, ExtraF ef) {
  int o[4];
#pragma unroll
  for (int t = 0; t < 4; ++t) o[t] = rbase ^ (t << 5);
  const bf16x8 ones = ones8();
  sfor<0, NK>([&](auto I) {
    constexpr int i = decltype(I)::value;
    wf(I);
    const bf16x8 af = asb(a[i]);
    if constexpr (LAG) {
      constexpr int j = i > 0 ? i - 1 : (NK < ROLLN ? NK : -1);
      if (!(SKIPLAST && i == NK - 1 && skip)) {
#pragma unroll
        for (int t = 0; t < 4; ++t) {
          acc[t] = mfma16(B[i][t], af, acc[t]);
          if constexpr (j >= 0 && j < ROLLN && !(SKIPLAST && i == NK - 1)) B[j][t] = tr_frag(smem, o[t] + j * 4096, o[t] + j * 4096 + 2048);
          SB
        }
      }
      if constexpr (SKIPLAST && i == NK - 1) {
#pragma unroll
        for (int t = 0; t < 4; ++t) B[j][t] = tr_frag(smem, o[t] + j * 4096, o[t] + j * 4096 + 2048);
      }
      if constexpr (i == NK - 1) {
#pragma unroll
        for (int jj = NK - 1; jj < ROLLN; ++jj) {
          if (jj == NK) continue;
#pragma unroll
          for (int t = 0; t < 4; ++t) B[jj][t] = tr_frag(smem, o[t] + jj * 4096, o[t] + jj * 4096 + 2048);
        }
      }
    } else {
#pragma unroll
      for (int t = 0; t < 4; ++t) acc[t] = mfma16(B[i][t], af, acc[t]);
      if constexpr (i < ROLLN) {
#pragma unroll
        for (int t = 0; t < 4; ++t) B[i][t] = tr_frag(smem, o[t] + i * 4096, o[t] + i * 4096 + 2048);
      }
      if constexpr (STATS) { ssum = dot8(af, ones, ssum); ssq = dot8(af, af, ssq); }
    }
    ef(I);
    SB
  });
}
template <int I0, int I1>
__device__ __forceinline__ void read_b(bf16x8 (&B)[8][4], const char* smem, int rbase) {
#pragma unroll
  for (int i = I0; i < I1; ++i)
#pragma unroll
    for (int t = 0; t < 4; ++t) {
      const int o = (rbase ^ (t << 5)) + i * 4096;
      B[i][t] = tr_frag(smem, o, o + 2048);
    }
}

__global__ __launch_bounds__(512, 2) void mega_k(const u16* __restrict__ wqkv, const u16* __restrict__ wphi,
                                                 const u16* __restrict__ wo, const u16* __restrict__ w1,
                                                 const u16* __restrict__ w2, const u16* __restrict__ w3,
                                                 const u16* __restrict__ w4, char* __restrict__ ws,
                                                 u16* __restrict__ out) {
  __shared__ __attribute__((aligned(1024))) char smem[L_ST + 1024];
  const int b = blockIdx.x;
  const int xcd = b & 7, idx = b >> 3;
  const int cl = xcd * 4 + (idx >> 3), nt = idx & 7;
  const int tid = threadIdx.x, w = __builtin_amdgcn_readfirstlane(tid >> 6), l0 = tid & 63;
  const int rt = w & 3, kh = w >> 2;
  const unsigned lb = lds_addr(smem);
  const u32x4 rs = mk_rsrc(ws, 0x10000000u);
  const __amdgpu_buffer_rsrc_t rst = __builtin_amdgcn_make_buffer_rsrc(ws, 0, 0x10000000, 0x00020000);
  const unsigned srow = (unsigned)(cl * 4 + rt);
  const unsigned sblk = (srow * 16 + nt * 2 + kh) * 1024;
  const int g3c = nt < 3 ? 32 + 2 * nt : 35 + nt;
  char* xs = smem + L_E + w * 2048;
  const char* xp = smem + L_E + (w ^ 4) * 2048;
  const float* csl = (const float*)(smem + L_CS);
  const f32x4 zf = {0.f, 0.f, 0.f, 0.f};
  const u32x4 pz = {0xffffffffu, 0xffffffffu, 0xffffffffu, 0xffffffffu};
  auto nohook = [](auto) {};

#define LANE                                                                    \
  const int l = opq(l0), r = l & 15, g = l >> 4;                                \
  const unsigned l16 = (unsigned)l * 16;                                        \
  const int wq = opqs(w);                                                       \
  auto dma = [&](const u16* W0, int ldw, unsigned vo, int p, int slot) {        \
    const int piece = p * 8 + wq;                                               \
    glds16((const char*)W0 + (unsigned)(piece * 16 * ldw), vo, lb + (unsigned)(slot + piece * 1024)); \
  };                                                                            \
  (void)r; (void)g; (void)l16; (void)dma;
#define WOFF ((((4 * g + (r >> 2)) * 128 + ((2 * (g & 1) + (r >> 3)) << 5) + (l & 3) * 8) ^ (kh << 6)))
#define DVOFF(ldw) ((unsigned)((l >> 3) * (ldw) * 2 + (((l & 7) ^ (((l >> 4) & 3) << 1)) << 4)))
#define ZACC _Pragma("unroll") for (int t = 0; t < 4; ++t) acc[t] = zf;
#define ALOAD(vo, rsrc, so) sfor<0, 8>([&](auto I) { constexpr int i = decltype(I)::value; bload<(i & 3) * 1024>(a0[i], vo, rsrc, so); });

  bf16x8 B[8][4];
  u32x4 a0[8];
  bf16x8 xres;
  {
    LANE
    const unsigned vq = DVOFF(1536);
#pragma unroll
    for (int p = 0; p < 8; ++p) dma(wqkv + 512 + nt * 64, 1536, vq, p, 0);
#pragma unroll
    for (int p = 0; p < 8; ++p) dma(wqkv + 1024 + nt * 64, 1536, vq, p, SLOT1);
    if (tid < 448) {
      const int u = tid >> 6;
      int c0 = 2048 + (u - 4) * 512 + nt * 64;
      if (u == 0) c0 = 512 + nt * 64;
      if (u == 1) c0 = 1024 + nt * 64;
      if (u == 2) c0 = nt * 64;
      if (u == 3) c0 = 1536 + nt * 64;
      if (u == 6) c0 = 2048 + g3c * 32;
      const float* cp = (const float*)(ws + WS_CS) + c0 + l;
      ((float*)(smem + L_CS))[tid] = (cp[0] + cp[3456]) + (cp[2 * 3456] + cp[3 * 3456]);
    } else {
      *(u32x4*)(smem + L_PHI + l * 32) = *(const u32x4*)(wphi + nt * 1024 + l * 16);
      *(u32x4*)(smem + L_PHI + l * 32 + 16) = *(const u32x4*)(wphi + nt * 1024 + l * 16 + 8);
    }
    xres = *(const bf16x8*)(ws + WS_XT + sblk + l16);
    wait_vm<0>();
    bar();
    read_b<0, 8>(B, smem, WOFF + kh * 32768);
    bar();
#pragma unroll
    for (int p = 0; p < 8; ++p) dma(wqkv + nt * 64, 1536, vq, p, 0);
  }

#pragma unroll 1
  for (int layer = 0; layer < 2; ++layer) {
    const unsigned lbase = WS_L0 + (unsigned)layer * WS_LSTR;
    const int sa = layer * SLOT1, sb = SLOT1 - sa;
    float d0, d1;
    f32x4 acc[4];
    ST(0)
    {
      LANE
      bstore(pz, l16, rst, lbase + O_H + sblk);
      bstore(pz, l16, rst, lbase + O_X1 + sblk);
      if (layer) nap<DL0>();
      const unsigned so = (layer ? WS_L0 + O_X2 : WS_XT) + srow * 16384 + kh * 8192;
      ALOAD(l16, rs, so + (i >> 2) * 4096)
      const int woff = WOFF + kh * 32768;
      wait_vm<18>();
      bar0();
      ST(1)
      float ssum = 0.f, ssq = 0.f;
      ZACC
      unit_loop<8, 8, true, false, false>(B, a0, acc, smem, woff + sb, ssum, ssq, false,
          [&](auto I) { constexpr int i = decltype(I)::value; wait_vm<7 - i>(a0[i]); fixup<(i & 3) * 1024, 0>(a0[i], l16, rs, so + (i >> 2) * 4096); },
          nohook);
      *(f32x4*)(xs + l16) = acc[2];
      *(f32x4*)(xs + 1024 + l16) = acc[3];
      ssum = xg_add(ssum); ssq = xg_add(ssq);
      if (g == 0) *(f32x2*)(smem + L_S + w * 128 + r * 8) = (f32x2){ssum, ssq};
      const int phoff = L_PHI + kh * 1024 + (4 * g + (r >> 2)) * 32 + (l & 3) * 8;
      const bf16x8 wp0 = pad8(tr4(smem, phoff)), wp1 = pad8(tr4(smem, phoff + 512));
      const float* cs0 = csl + 32 * kh + 4 * g;
      float mean, rstd;
      {
        const f32x4 c0 = *(const f32x4*)cs0, c1 = *(const f32x4*)(cs0 + 16);
        ST(2)
        bar();
        const f32x4 x0 = *(const f32x4*)(xp + l16), x1 = *(const f32x4*)(xp + 1024 + l16);
        const f32x2 sp = *(const f32x2*)(smem + L_S + (w ^ 4) * 128 + r * 8);
        SB
        mean = (ssum + sp[0]) * (1.0f / DM);
        float var = (ssq + sp[1]) * (1.0f / DM) - mean * mean;
        var = var < 0.f ? 0.f : var;
        rstd = __builtin_amdgcn_rsqf(var + 1e-5f);
        const f32x4 k0 = (acc[0] + x0 - mean * c0) * rstd, k1 = (acc[1] + x1 - mean * c1) * rstd;
        const f32x4 p0 = mfma16(wp0, pad8(cvt4(k0)), zf), p1 = mfma16(wp1, pad8(cvt4(k1)), zf);
        u32x4 f0, f1;
        dual_softmax2(p0, p1, f0, f1, nohook);
        char* trow = smem + sb + T_PHI + (16 * rt + r) * 288 + kh * 128 + g * 8;
        *(u32x2*)(trow) = (u32x2){f0[0], f0[1]};
        *(u32x2*)(trow + 32) = (u32x2){f0[2], f0[3]};
        *(u32x2*)(trow + 64) = (u32x2){f1[0], f1[1]};
        *(u32x2*)(trow + 96) = (u32x2){f1[2], f1[3]};
      }
      ST(3)
      ZACC
      unit_loop<8, 8, false, false, true>(B, a0, acc, smem, woff + sa, d0, d1, false, nohook, nohook);
      {
        char* xs2 = smem + sb + T_E + w * 2048;
        const char* xp2 = smem + sb + T_E + (w ^ 4) * 2048;
        *(f32x4*)(xs2 + l16) = acc[2];
        *(f32x4*)(xs2 + 1024 + l16) = acc[3];
        const f32x4 c0 = *(const f32x4*)(cs0 + 64), c1 = *(const f32x4*)(cs0 + 80);
        ST(4)
        bar();
        const f32x4 x0 = *(const f32x4*)(xp2 + l16), x1 = *(const f32x4*)(xp2 + 1024 + l16);
        SB
        char* trow = smem + sb + T_V + (16 * rt + r) * 160 + kh * 64 + g * 8;
        *(bf16x4*)(trow) = cvt4((acc[0] + x0 - mean * c0) * rstd);
        *(bf16x4*)(trow + 32) = cvt4((acc[1] + x1 - mean * c1) * rstd);
      }
      {
        const int hh = w >> 1, ft = w & 1;
        const int rr = 4 * g + (r >> 2);
        const int oa = sb + T_PHI + rr * 288 + hh * 64 + ft * 32 + (l & 3) * 8;
        const int ob = sb + T_V + rr * 160 + hh * 32 + (l & 3) * 8;
        const bf16x8 ones = ones8();
        const bf16x8 pa0 = tr_frag(smem, oa, oa + 16 * 288), pa1 = tr_frag(smem, oa + 32 * 288, oa + 48 * 288);
        bar();
        const bf16x8 vb0 = tr_frag(smem, ob, ob + 16 * 160), vb1 = tr_frag(smem, ob + 32 * 160, ob + 48 * 160);
        const f32x4 z = mfma16(pa1, ones, mfma16(pa0, ones, zf));
        const f32x4 kv = mfma16(pa1, vb1, mfma16(pa0, vb0, zf));
        const unsigned rec = lbase + O_KVP + (unsigned)((nt * 4 + hh) * 32 + cl) * KVREC;
        bstore(fsu(kv), (unsigned)((l >> 3) * 256 + (l & 7) * 16), rst, rec + ft * 128);
        if (r == 0) bstore(fsu(z), (unsigned)g * 16, rst, rec + 2048 + ft * 128);
      }
      ST(5)
      const unsigned v5 = DVOFF(512);
      ZACC
      unit_loop<8, 0, false, false, false>(B, a0, acc, smem, woff, d0, d1, false, nohook,
          [&](auto I) {
            constexpr int i = decltype(I)::value;
            if constexpr (i < 4) { dma(wo + nt * 64, 512, v5, 2 * i, sa); dma(wo + nt * 64, 512, v5, 2 * i + 1, sa); }
          });
      *(f32x4*)(xs + l16) = acc[2];
      *(f32x4*)(xs + 1024 + l16) = acc[3];
      const bool red = w == 0 || (w == 1 && nt == 0);
      const unsigned sr = lbase + O_KVP + (unsigned)cl * 32 * KVREC;
      const unsigned vr = w == 0 ? (unsigned)g * KVREC + (unsigned)(nt * 256 + r * 16)
                                 : (unsigned)g * KVREC + 2048u + (unsigned)((r >> 3) * 128 + (r & 3) * 16);
      if (red) sfor<0, 8>([&](auto I) { constexpr int i = decltype(I)::value; bload<0>(a0[i], vr, rs, sr + i * 4 * KVREC); });
      u32x4 phq0, phq1;
      {
        const f32x4 c0 = *(const f32x4*)(cs0 + 128), c1 = *(const f32x4*)(cs0 + 144);
        ST(6)
        bar();
        const f32x4 x0 = *(const f32x4*)(xp + l16), x1 = *(const f32x4*)(xp + 1024 + l16);
        SB
        dma(w1 + nt * 64, 512, v5, 0, sb);
        SB
        const f32x4 q0 = (acc[0] + x0 - mean * c0) * rstd, q1 = (acc[1] + x1 - mean * c1) * rstd;
        const f32x4 p0 = mfma16(wp0, pad8(cvt4(q0)), zf), p1 = mfma16(wp1, pad8(cvt4(q1)), zf);
        SB
        dma(w1 + nt * 64, 512, v5, 7, sb);
        dual_softmax2(p0, p1, phq0, phq1, [&](auto I) { dma(w1 + nt * 64, 512, v5, decltype(I)::value, sb); });
      }
      ST(7)
      wait_vm<8>();
      if (red) {
        unsigned n = 0;
        for (;;) {
          unsigned m = 0;
#pragma unroll
          for (int i = 0; i < 8; ++i) { tie(a0[i]); const unsigned m2 = max4(a0[i]); m = m > m2 ? m : m2; }
          if (!__any(m == 0xffffffffu) || ++n >= SPIN_CAP) break;
          CNT(32)
          sfor<0, 8>([&](auto I) { constexpr int i = decltype(I)::value; bload<0>(a0[i], vr, rs, sr + i * 4 * KVREC); });
          wait_vm<0>();
        }
        f32x4 s = ((asf(a0[0]) + asf(a0[1])) + (asf(a0[2]) + asf(a0[3]))) + ((asf(a0[4]) + asf(a0[5])) + (asf(a0[6]) + asf(a0[7])));
#pragma unroll
        for (int j = 0; j < 4; ++j) s[j] = xg_add(s[j]);
        const int u0 = (int)pk2(s[0], s[1]), u1 = (int)pk2(s[2], s[3]);
        const int y0 = __builtin_amdgcn_update_dpp(u0, u0, 0x128, 0xf, 0xf, false);
        const int y1 = __builtin_amdgcn_update_dpp(u1, u1, 0x128, 0xf, 0xf, false);
        const u32x4 o4 = {(unsigned)u0, (unsigned)u1, (unsigned)y0, (unsigned)y1};
        const unsigned kr = lbase + O_KVR + (unsigned)cl * KVR + (w == 0 ? (unsigned)nt * 128 : 1024u);
        if (l < (w == 0 ? 8 : 4)) bstore(o4, l16, rst, kr);
      }
      ST(8)
      bar0();
      read_b<0, 4>(B, smem, woff + sa);
      SB
      nap<DLK>();
      const unsigned kr = lbase + O_KVR + (unsigned)(nt * 4 + rt) * KVR;
      const unsigned vg = 1024u + (unsigned)(l & 3) * 16;
      if (kh) {
        asm volatile("s_waitcnt lgkmcnt(0)" ::: "memory");
        bload<0>(a0[0], l16, rs, kr);
        bload<0>(a0[1], vg, rs, kr);
      }
      SB
      read_b<4, 8>(B, smem, woff + sa);
      SB
      ST(9)
      wait_vm<0>();
      if (kh) {
        unsigned n = 0;
        for (;;) {
          tie(a0[0]); tie(a0[1]);
          if (!(poisoned(a0[0]) || poisoned(a0[1])) || ++n >= SPIN_CAP) break;
          CNT(33)
          nap<1>();
          bload<0>(a0[0], l16, rs, kr);
          bload<0>(a0[1], vg, rs, kr);
          wait_vm<0>();
        }
        *(u32x4*)(smem + L_KV + rt * 1088 + l16) = a0[0];
        if (l < 4) *(u32x4*)(smem + L_KV + rt * 1088 + 1024 + l16) = a0[1];
      }
      bar();
      ST(10)
      {
        const char* kp = smem + L_KV + kh * 2176;
        const bf16x8 kv0 = *(const bf16x8*)(kp + l16), z0 = *(const bf16x8*)(kp + 1024 + g * 16);
        const bf16x8 kv1 = *(const bf16x8*)(kp + 1088 + l16), z1 = *(const bf16x8*)(kp + 1088 + 1024 + g * 16);
        SB
        const f32x4 dn0 = mfma16(z0, asb(phq0), zf), dn1 = mfma16(z1, asb(phq1), zf);
        const f32x4 n0 = mfma16(kv0, asb(phq0), zf), n1 = mfma16(kv1, asb(phq1), zf);
        const float r0 = __builtin_amdgcn_rcpf(bfr(bfr(dn0[0]) + 1e-6f));
        const float r1 = __builtin_amdgcn_rcpf(bfr(bfr(dn1[0]) + 1e-6f));
        bstore(asu(cat8(cvt4(bfr4(n0) * r0), cvt4(bfr4(n1) * r1))), l16, rst, lbase + O_O + sblk);
      }
    }
    ST(11)
    bf16x8 x1;
    {
      LANE
      const int woff = WOFF + kh * 32768;
      const unsigned v5 = DVOFF(512);
      bstore(pz, l16, rst, lbase + O_Y1 + sblk);
#pragma unroll
      for (int p = 0; p < 8; ++p) dma(w2 + nt * 64, 512, v5, p, sa);
      nap<DL3>();
      const unsigned so = lbase + O_O + srow * 16384 + kh * 8192;
      ALOAD(l16, rs, so + (i >> 2) * 4096)
      ST(12)
      ZACC
      unit_loop<8, 8, false, false, false>(B, a0, acc, smem, woff + sb, d0, d1, false,
          [&](auto I) { constexpr int i = decltype(I)::value; wait_vm<7 - i>(a0[i]); fixup<(i & 3) * 1024, 1>(a0[i], l16, rs, so + (i >> 2) * 4096); },
          nohook);
      *(f32x4*)(xs + l16) = acc[2];
      *(f32x4*)(xs + 1024 + l16) = acc[3];
      xres = opq8(xres);
      const f32x4 r0 = up4(lo4(xres)), r1 = up4(hi4(xres));
      ST(13)
      bar();
      const f32x4 x0 = *(const f32x4*)(xp + l16), xx1 = *(const f32x4*)(xp + 1024 + l16);
      SB
      x1 = cat8(cvt4(r0 + bfr4(acc[0] + x0)), cvt4(r1 + bfr4(acc[1] + xx1)));
      bstore(asu(x1), l16, rst, lbase + O_X1 + sblk);
    }
    ST(14)
    const unsigned h2b = lbase + O_H2 + srow * 43 * 1024;
    const unsigned h2c = (kh == 0 || nt < 3) ? h2b + (unsigned)(g3c + kh) * 1024 : lbase + O_JUNK + (sblk >> 11) * 1024;
    bf16x8 xn;
    {
      LANE
      const int woff = WOFF + kh * 32768;
      const unsigned v3 = DVOFF(FF);
      bstore(pz, l16, rst, h2b + (nt * 2 + kh) * 1024);
      bstore(pz, l16, rst, h2b + (16 + nt * 2 + kh) * 1024);
      bstore(pz, l16, rst, h2c);
#pragma unroll
      for (int p = 0; p < 8; ++p) dma(w3 + nt * 64, FF, v3, p, sb);
      nap<DL4>();
      const unsigned so = lbase + O_X1 + srow * 16384 + kh * 8192;
      ALOAD(l16, rs, so + (i >> 2) * 4096)
      bar0();
      ST(15)
      float ssum = 0.f, ssq = 0.f;
      ZACC
      unit_loop<8, 8, true, false, false>(B, a0, acc, smem, woff + sa, ssum, ssq, false,
          [&](auto I) { constexpr int i = decltype(I)::value; wait_vm<7 - i>(a0[i]); fixup<(i & 3) * 1024, 2>(a0[i], l16, rs, so + (i >> 2) * 4096); },
          nohook);
      *(f32x4*)(xs + l16) = acc[2];
      *(f32x4*)(xs + 1024 + l16) = acc[3];
      ssum = xg_add(ssum); ssq = xg_add(ssq);
      if (g == 0) *(f32x2*)(smem + L_S + w * 128 + r * 8) = (f32x2){ssum, ssq};
      const float* cs0 = csl + 192 + 32 * kh + 4 * g;
      const f32x4 c0 = *(const f32x4*)cs0, c1 = *(const f32x4*)(cs0 + 16);
      ST(16)
      bar();
      const f32x4 x0 = *(const f32x4*)(xp + l16), xx1 = *(const f32x4*)(xp + 1024 + l16);
      const f32x2 sp = *(const f32x2*)(smem + L_S + (w ^ 4) * 128 + r * 8);
      SB
      const float mean = (ssum + sp[0]) * (1.0f / DM);
      float var = (ssq + sp[1]) * (1.0f / DM) - mean * mean;
      var = var < 0.f ? 0.f : var;
      const float rstd = __builtin_amdgcn_rsqf(var + 1e-5f);
      const float scale = rstd * __builtin_amdgcn_rsqf(var * rstd * rstd + 1e-5f);
      const u32x4 hv = gelu8((acc[0] + x0 - mean * c0) * scale, (acc[1] + xx1 - mean * c1) * scale, nohook);
      bstore(hv, l16, rst, lbase + O_H + sblk);
      SB
      x1 = opq8(x1);
      xn = cat8(cvt4((up4(lo4(x1)) - mean) * rstd), cvt4((up4(hi4(x1)) - mean) * rstd));
    }
    ST(17)
    bf16x8 y1;
    {
      LANE
      const int woff = WOFF + kh * 32768;
      const unsigned v3 = DVOFF(FF);
      bstore(pz, l16, rst, lbase + O_X2 + sblk);
#pragma unroll
      for (int p = 0; p < 8; ++p) dma(w3 + 512 + nt * 64, FF, v3, p, sa);
      nap<DL5>();
      const u32x4 rh = mk_rsrc(ws + lbase + O_H, 2u << 20);
      const int s0 = (int)srow * 16 + r - (1 + kh), s1 = (int)srow * 16 + r + (1 + kh);
      const unsigned v0 = (unsigned)((s0 >> 4) * 16384 + kh * 8192 + ((s0 & 15) + 16 * g) * 16);
      const unsigned v1 = (unsigned)((s1 >> 4) * 16384 + kh * 8192 + 4096 + ((s1 & 15) + 16 * g) * 16);
      const unsigned z0 = 0u;
      ALOAD((i < 4 ? v0 : v1), rh, z0)
      bar0();
      ST(18)
      ZACC
      unit_loop<8, 8, false, false, false>(B, a0, acc, smem, woff + sb, d0, d1, false,
          [&](auto I) { constexpr int i = decltype(I)::value; wait_vm<7 - i>(a0[i]); fixup<(i & 3) * 1024, 3>(a0[i], i < 4 ? v0 : v1, rh, z0); },
          nohook);
      *(f32x4*)(xs + l16) = acc[2];
      *(f32x4*)(xs + 1024 + l16) = acc[3];
      xn = opq8(xn);
      const f32x4 r0 = up4(lo4(xn)), r1 = up4(hi4(xn));
      ST(19)
      bar();
      const f32x4 x0 = *(const f32x4*)(xp + l16), xx1 = *(const f32x4*)(xp + 1024 + l16);
      SB
      y1 = cat8(cvt4(r0 + bfr4(acc[0] + x0)), cvt4(r1 + bfr4(acc[1] + xx1)));
      bstore(asu(y1), l16, rst, lbase + O_Y1 + sblk);
    }
    ST(20)
    {
      LANE
      const int woff = WOFF + kh * 32768;
      {
        const unsigned v3c = (unsigned)(g3c * 64 + (l >> 3) * FF * 2 + ((((l & 7) ^ (((l >> 4) & 3) << 1)) & (nt < 3 ? 7 : 3)) << 4));
#pragma unroll
        for (int p = 0; p < 8; ++p) dma(w3, FF, v3c, p, sb);
      }
      nap<DL6>();
      const unsigned so = lbase + O_Y1 + srow * 16384 + kh * 8192;
      ALOAD(l16, rs, so + (i >> 2) * 4096)
      bar0();
      ST(21)
      float ssum = 0.f, ssq = 0.f;
      ZACC
      unit_loop<8, 8, true, false, false>(B, a0, acc, smem, woff + sa, ssum, ssq, false,
          [&](auto I) { constexpr int i = decltype(I)::value; wait_vm<7 - i>(a0[i]); fixup<(i & 3) * 1024, 4>(a0[i], l16, rs, so + (i >> 2) * 4096); },
          nohook);
      *(f32x4*)(xs + l16) = acc[2];
      *(f32x4*)(xs + 1024 + l16) = acc[3];
      ssum = xg_add(ssum); ssq = xg_add(ssq);
      if (g == 0) *(f32x2*)(smem + L_S + w * 128 + r * 8) = (f32x2){ssum, ssq};
      const float* cs0 = csl + 256 + 32 * kh + 4 * g;
      f32x4 c0 = *(const f32x4*)cs0, c1 = *(const f32x4*)(cs0 + 16);
      ST(22)
      bar();
      const f32x2 sp = *(const f32x2*)(smem + L_S + (w ^ 4) * 128 + r * 8);
      float mean, rstd;
      auto epi3 = [&](auto first, unsigned dst, auto hk) {
        const f32x4 x0 = *(const f32x4*)(xp + l16), xx1 = *(const f32x4*)(xp + 1024 + l16);
        SB
        hk(IC<0>{});
        SB
        if constexpr (decltype(first)::value) {
          mean = (ssum + sp[0]) * (1.0f / DM);
          float var = (ssq + sp[1]) * (1.0f / DM) - mean * mean;
          var = var < 0.f ? 0.f : var;
          rstd = __builtin_amdgcn_rsqf(var + 1e-5f);
        }
        const u32x4 hv = gelu8((acc[0] + x0 - mean * c0) * rstd, (acc[1] + xx1 - mean * c1) * rstd, hk);
        bstore(hv, l16, rst, dst);
        SB
        hk(IC<7>{});
        ZACC
      };
      const unsigned v5 = DVOFF(512);
      const u16* w4c = w4 + nt * 64;
      epi3(IC<1>{}, h2b + (nt * 2 + kh) * 1024, [&](auto I) { dma(w4c, 512, v5, decltype(I)::value, sa); });
      ST(23)
      unit_loop<8, 8, false, false, true>(B, a0, acc, smem, woff + sb, d0, d1, false, nohook,
          [&](auto I) { if constexpr (decltype(I)::value == 3) { SB bar0(); } });
      *(f32x4*)(xs + l16) = acc[2];
      *(f32x4*)(xs + 1024 + l16) = acc[3];
      c0 = *(const f32x4*)(cs0 + 64); c1 = *(const f32x4*)(cs0 + 80);
      wait_vm<0>();
      ST(24)
      bar();
      epi3(IC<0>{}, h2b + (16 + nt * 2 + kh) * 1024, [&](auto I) { dma(w4c + 512 * 512, 512, v5, decltype(I)::value, sb); });
      ST(25)
      const unsigned sh = h2b + kh * 8192;
      unit_loop<8, 8, false, false, true>(B, a0, acc, smem, woff + sa, d0, d1, false, nohook,
          [&](auto I) {
            constexpr int i = decltype(I)::value;
            bload<(i & 3) * 1024>(a0[i], l16, rs, sh + (i >> 2) * 4096);
            if constexpr (i == 3) { SB bar0(); }
          });
      *(f32x4*)(xs + l16) = acc[2];
      *(f32x4*)(xs + 1024 + l16) = acc[3];
      c0 = *(const f32x4*)(cs0 + 128); c1 = *(const f32x4*)(cs0 + 144);
      wait_vm<8>();
      ST(26)
      bar();
      epi3(IC<0>{}, h2c, [&](auto I) {
        constexpr int p = decltype(I)::value;
        if constexpr (p < 6) {
          const int pa = p * 8 + wq, sp_ = pa < 44 ? pa : 43;
          glds16((const char*)(w4c + 1024 * 512) + (unsigned)(sp_ * 8192), v5, lb + (unsigned)(sa + pa * 1024));
        }
      });
      ST(27)
      unit_loop<8, 8, false, false, true>(B, a0, acc, smem, woff + sb, d0, d1, false,
          [&](auto I) { constexpr int i = decltype(I)::value; wait_vm<14>(a0[i]); fixup<(i & 3) * 1024, 5>(a0[i], l16, rs, sh + (i >> 2) * 4096); },
          [&](auto I) { constexpr int i = decltype(I)::value; bload<(i & 3) * 1024>(a0[i], l16, rs, sh + 16384 + (i >> 2) * 4096); });
      wait_vm<9>();
      ST(28)
      bar();
      const unsigned vq = layer == 0 ? DVOFF(1536) : 0u;
      const int ldq = layer == 0 ? 1536 : 0;
      const unsigned sc = h2b + (32 + kh * 6) * 1024;
      const unsigned sc5 = h2b + (37 + kh * 5) * 1024;
      unit_loop<8, 6, false, false, true>(B, a0, acc, smem, WOFF + kh * 24576 + sa, d0, d1, false,
          [&](auto I) { constexpr int i = decltype(I)::value; wait_vm<(i < 5 ? 7 + 2 * i : (i < 7 ? 15 : 14))>(a0[i]); fixup<(i & 3) * 1024, 6>(a0[i], l16, rs, sh + 16384 + (i >> 2) * 4096); },
          [&](auto I) {
            constexpr int i = decltype(I)::value;
            if constexpr (i < 4) { dma(wqkv + 512 + nt * 64, ldq, vq, 2 * i, sb); dma(wqkv + 512 + nt * 64, ldq, vq, 2 * i + 1, sb); }
            if constexpr (i < 5) bload<(i & 3) * 1024>(a0[i], l16, rs, sc + (i >> 2) * 4096);
            if constexpr (i == 5) bload<0>(a0[5], l16, rs, sc5);
          });
      wait_vm<3>();
      ST(29)
      bar();
      unit_loop<6, 8, false, true, true>(B, a0, acc, smem, woff + sb, d0, d1, kh != 0,
          [&](auto I) {
            constexpr int i = decltype(I)::value;
            wait_vm<(i < 5 ? 5 + i : 8)>(a0[i]);
            if constexpr (i < 5) fixup<(i & 3) * 1024, 7>(a0[i], l16, rs, sc + (i >> 2) * 4096);
            else fixup<0, 8>(a0[i], l16, rs, sc5);
          },
          [&](auto I) {
            constexpr int i = decltype(I)::value;
            if constexpr (i < 4) { dma(wqkv + 1024 + nt * 64, ldq, vq, 2 * i, sa); dma(wqkv + 1024 + nt * 64, ldq, vq, 2 * i + 1, sa); }
          });
      *(f32x4*)(xs + l16) = acc[2];
      *(f32x4*)(xs + 1024 + l16) = acc[3];
      x1 = opq8(x1); y1 = opq8(y1);
      const f32x4 ra0 = up4(lo4(x1)), ra1 = up4(hi4(x1)), rb0 = up4(lo4(y1)), rb1 = up4(hi4(y1));
      ST(30)
      bar();
      const f32x4 x0 = *(const f32x4*)(xp + l16), xx1 = *(const f32x4*)(xp + 1024 + l16);
      SB
      const bf16x4 r0 = cvt4(ra0 + bfr4(rb0 + bfr4(acc[0] + x0)));
      const bf16x4 r1 = cvt4(ra1 + bfr4(rb1 + bfr4(acc[1] + xx1)));
      xres = cat8(r0, r1);
      if (layer == 0) {
        bstore(asu(xres), l16, rst, lbase + O_X2 + sblk);
      } else {
        u16* op = out + (size_t)(srow * 16 + r) * DM + (nt * 2 + kh) * 32 + 4 * g;
        *(bf16x4*)op = r0;
        *(bf16x4*)(op + 16) = r1;
      }
#pragma unroll
      for (int p = 0; p < 8; ++p) dma(wqkv + nt * 64, ldq, vq, p, sb);
    }
    ST(31)
  }
  wait_vm<0>();
}

extern "C" void kernel_launch(void* const* d_in, const int* in_sizes, int n_in, void* d_out, int out_size,
                              void* d_ws, size_t ws_size, hipStream_t stream) {
  const u16* x0 = (const u16*)d_in[0];
  const u16* wqkv = (const u16*)d_in[1];
  const u16* wphi = (const u16*)d_in[2];
  const u16* wo = (const u16*)d_in[3];
  const u16* w1 = (const u16*)d_in[5];
  const u16* w2 = (const u16*)d_in[6];
  const u16* w3 = (const u16*)d_in[7];
  const u16* w4 = (const u16*)d_in[8];
  char* ws = (char*)d_ws;
  setup_k<<<256, 256, 0, stream>>>(x0, wqkv, w1, w3, ws);
  mega_k<<<256, 512, 0, stream>>>(wqkv, wphi, wo, w1, w2, w3, w4, ws, (u16*)d_out);
}
```

```cpp
#include <hip/hip_runtime.h>
#include <stdint.h>

typedef __attribute__((ext_vector_type(8))) __bf16 bf16x8;
typedef __attribute__((ext_vector_type(4))) __bf16 bf16x4;
typedef __attribute__((ext_vector_type(2))) __bf16 bf16x2;
typedef __attribute__((ext_vector_type(4))) float f32x4;
typedef __attribute__((ext_vector_type(2))) float f32x2;
typedef __attribute__((ext_vector_type(4))) unsigned u32x4;
typedef __attribute__((ext_vector_type(2))) unsigned u32x2;
typedef unsigned short u16;

#define LDSP(T, p) ((__attribute__((address_space(3))) T*)(p))
#define VL(T, off) (*(volatile __attribute__((address_space(3))) T*)(LDSP(char, smem) + (off)))
#define SB __builtin_amdgcn_sched_barrier(0);

static constexpr int DM = 512;
static constexpr int FF = 1376;
static constexpr unsigned KVREC = 2176;
static constexpr unsigned KVR = 1152;
static constexpr unsigned WS_CS = 0;
static constexpr unsigned WS_P = 256u << 10;
static constexpr unsigned WS_KVP = 1u << 20;
static constexpr unsigned MB2 = 2u << 20;
static constexpr unsigned WS_B = 4u << 20;
static constexpr unsigned WS_H = WS_B + 5 * MB2, WS_C = WS_H + MB2;
static constexpr unsigned WS_SA = WS_C + MB2, WS_SB = WS_SA + (256u << 10), WS_SC = WS_SB + (256u << 10), WS_KVR = WS_SC + (256u << 10);
static constexpr unsigned WS_BYTES = 0x08000000u;
static constexpr unsigned OOB = 0x7ff00000u;
static constexpr int L_EX = 8 * 16384;
static constexpr int L_V = L_EX + 16384;
static constexpr int L_WPH = L_V + 8192;
static constexpr int L_CS = L_WPH + 2048;
static constexpr int L_F = L_CS + 2048;
static constexpr int F_LAND = L_F + 256, F_CS = L_F, F_KV = L_F + 8, F_PD = L_F + 16, F_PROG = L_F + 32, F_X = L_F + 64, F_DEP = L_F + 96, F_DUM = L_F + 128, F_PZ = L_F + 24;
static constexpr int L_ST = L_F + 768;
static constexpr int L_END = L_ST;
#define SPINCAP 2000000
#define PACE 0
#define DLY 32
#define D0A DLY
#define D0B D0A
#define D0C D0A
#define D0D D0A
#define DLX DLY
#define GATE 2
#define RA 0
#define RAD 6
#define OREL 1
#define NAPL(a, b)                                                     \
  if constexpr (GATE == 1) lds_put(lb + F_PROG + w * 4, un);           \
  if constexpr (RA > 0) { nap<RA>(); readall(); release(); nap<a - RA - RAD>(); } else nap<a>();
#define RNAP 2
#define PRA 2
#define PRB 2
#define PZ1 8
#define PZ2 5
#define GM 2
#define CSL 6
#define DP0 16
#define NPK 0
#if defined(__HIP_DEVICE_COMPILE__) && (NPK & 1)
#define NOPK1 __attribute__((target("no-packed-fp32-ops")))
#else
#define NOPK1
#endif
#if defined(__HIP_DEVICE_COMPILE__) && (NPK & 2)
#define NOPK2 __attribute__((target("no-packed-fp32-ops")))
#else
#define NOPK2
#endif
#define THW 6
#define THM 0x0f8
#define THM1 0
#define TT 0
#define PZK 0
#define PZ3 4
#define PZ30 0
#define HT 0
#define KT 60
#define PIN 7
#define NPART 1
#define SLD 1
#define PZ0 0
#define LIN 3
#define DQ1 0
#define LZN 0
#define OT 0
#define OTI 18
#define PK 3
#define QX 1
#define DKH 0
#define QP 3
#define XB 0
#define K2B 1
#define P1A 1
#define P1B 1
#define XROT 2
#define XN 0
#define KTN 0
#define PFI 3
#define HN 24
#define KN 2
#define LK 2
#define HS 7
#define DLR 12
#define DLK 8

#define SQ8(b) b + 1, b + 2, b + 3, b + 4, b + 5, b + 6, b + 7, b + 8
__device__ const unsigned c_seq[48] = {SQ8(0), SQ8(8), SQ8(16), SQ8(24), SQ8(32), SQ8(40)};

#define ST(i)
#define STF
#define STD(i)
#define STR(i)
#define CNTR
#define STC(i)
#define SF(i)

__device__ __forceinline__ f32x4 mfma16(bf16x8 a, bf16x8 b, f32x4 c) {
  return __builtin_amdgcn_mfma_f32_16x16x32_bf16(a, b, c, 0, 0, 0);
}
__device__ __forceinline__ bf16x4 tr4(const char* smem, int off) {
  return __builtin_amdgcn_ds_read_tr16_b64_v4bf16(LDSP(bf16x4, smem + off));
}
__device__ __forceinline__ bf16x8 tr_frag(const char* smem, int off0, int off1) {
  bf16x4 v0 = tr4(smem, off0);
  bf16x4 v1 = tr4(smem, off1);
  return __builtin_shufflevector(v0, v1, 0, 1, 2, 3, 4, 5, 6, 7);
}
__device__ __forceinline__ void glds16(const void* base, unsigned voff, unsigned lds) {
  unsigned keep;
  asm volatile("s_mov_b32 %0, m0\n\ts_mov_b32 m0, %3\n\ts_nop 0\n\tglobal_load_lds_dwordx4 %1, %2\n\ts_mov_b32 m0, %0"
               : "=&s"(keep) : "v"(voff), "s"(base), "s"(lds) : "memory");
}
#define DP1 "buffer_load_dwordx4 %2, %3, %0 offen lds\n\ts_add_u32 m0, m0, 0x400\n\ts_add_u32 %0, %0, %4\n\t"
#define DP4 DP1 DP1 DP1 DP1
#define DHEAD "s_mov_b32 %1, m0\n\ts_mov_b32 m0, %5\n\ts_nop 0\n\t"
#define DTAIL "s_mov_b32 m0, %8\n\ts_nop 0\n\tbuffer_load_dword off, %6, %7 lds\n\ts_mov_b32 m0, %1"
#define DARGS : "+s"(soff), "=&s"(keep) : "v"(voff), "s"(rs), "s"(pst), "s"(lds), "s"(rq), "s"(sq), "s"(lf) : "memory", "scc"
template <int NP>
__device__ __forceinline__ void dma_unit(unsigned voff, u32x4 rs, unsigned soff, unsigned pst, unsigned lds, u32x4 rq, unsigned sq, unsigned lf) {
  unsigned keep;
  if constexpr (NP == 16) asm volatile(DHEAD DP4 DP4 DP4 DP4 DTAIL DARGS);
#define STR_(x) #x
#define STR2(x) STR_(x)
#define DW "s_waitcnt vmcnt(" STR2(THW) ")\n\t"
#define DP2 DP1 DP1
  if constexpr (NP == 17) asm volatile(DHEAD DW DP2 DW DP2 DW DP2 DW DP2 DW DP2 DW DP2 DW DP2 DW DP2 DTAIL DARGS);
  if constexpr (NP == 8) asm volatile(DHEAD DP4 DP4 DTAIL DARGS);
  if constexpr (NP == 4) asm volatile(DHEAD DP4 DTAIL DARGS);
  if constexpr (NP == 1) asm volatile(DHEAD DP1 DTAIL DARGS);
}
__device__ __forceinline__ unsigned lds_addr(const char* p) { return (unsigned)(uintptr_t)LDSP(const char, p); }
template <int N>
__device__ __forceinline__ void wait_vm() {
  asm volatile("s_waitcnt vmcnt(%0)" ::"n"(N) : "memory");
}
template <int N>
__device__ __forceinline__ void wait_vm(u32x4& a) {
  asm volatile("s_waitcnt vmcnt(%1)" : "+v"(a) : "n"(N) : "memory");
}
__device__ __forceinline__ void tie(u32x4& a) { asm volatile("" : "+v"(a)::"memory"); }
template <int N>
__device__ __forceinline__ void nap() {
  if constexpr (N > 0) asm volatile("s_sleep %0" ::"n"(N) : "memory");
}
template <int IMM>
__device__ __forceinline__ void bload(u32x4& d, unsigned voff, u32x4 rs, unsigned soff) {
  asm volatile("buffer_load_dwordx4 %0, %1, %2, %3 offen offset:%4 sc1"
               : "=v"(d) : "v"(voff), "s"(rs), "s"(soff), "n"(IMM) : "memory");
}
template <int IMM>
__device__ __forceinline__ void pload(u32x4& d, unsigned voff, u32x4 rs) {
  asm volatile("buffer_load_dwordx4 %0, %1, %2, 0 offen offset:%3" : "=v"(d) : "v"(voff), "s"(rs), "n"(IMM) : "memory");
}
__device__ __forceinline__ void touch(unsigned& d, unsigned voff, u32x4 rs) {
  asm volatile("buffer_load_dword %0, %1, %2, 0 offen" : "+v"(d) : "v"(voff), "s"(rs) : "memory");
}
__device__ __forceinline__ void touch1(unsigned& d, unsigned voff, u32x4 rs) {
  asm volatile("buffer_load_dword %0, %1, %2, 0 offen sc1" : "+v"(d) : "v"(voff), "s"(rs) : "memory");
}
template <int IMM>
__device__ __forceinline__ void breload(u32x4& d, unsigned voff, u32x4 rs, unsigned soff) {
  asm volatile("buffer_load_dwordx4 %0, %1, %2, %3 offen offset:%4 sc1"
               : "+v"(d) : "v"(voff), "s"(rs), "s"(soff), "n"(IMM) : "memory");
}
__device__ __forceinline__ void bstore(u32x4 d, unsigned voff, u32x4 rs, unsigned soff) {
  asm volatile("s_nop 4\n\tbuffer_store_dwordx4 %0, %1, %2, %3 offen sc1\n\ts_nop 1"
               ::"v"(d), "v"(voff), "s"(rs), "s"(soff) : "memory");
}
__device__ __forceinline__ void bstore8(u32x2 d, unsigned voff, u32x4 rs, unsigned soff) {
  asm volatile("s_nop 4\n\tbuffer_store_dwordx2 %0, %1, %2, %3 offen sc1\n\ts_nop 1"
               ::"v"(d), "v"(voff), "s"(rs), "s"(soff) : "memory");
}
__device__ __forceinline__ unsigned badbits(u32x4 a, unsigned t) { return (a[0] ^ t) | (a[2] ^ t); }
__device__ __forceinline__ bool bad(u32x4 a, unsigned t) { return __any(badbits(a, t) & 1u); }
__device__ __forceinline__ u32x4 tagged(u32x4 a, unsigned t) {
  a[0] = (a[0] & ~1u) | t; a[2] = (a[2] & ~1u) | t;
  return a;
}
__device__ __forceinline__ u32x4 mk_rsrc(const void* p, unsigned bytes) {
  const unsigned long long a = (unsigned long long)p;
  u32x4 r;
  r[0] = (unsigned)a; r[1] = (unsigned)(a >> 32) & 0xffffu; r[2] = bytes; r[3] = 0x00020000u;
  return r;
}
#define K2L 49152
static constexpr unsigned K2LEN = K2L;
__device__ __forceinline__ u32x4 code_rsrc(unsigned& k2off) {
  unsigned long long pc; int d1;
  asm volatile("s_getpc_b64 %0\n.Lpc%=:\n\ts_mov_b32 %1, hk1-.Lpc%=\n\ts_mov_b32 %2, hk2-hk1" : "=s"(pc), "=s"(d1), "=s"(k2off));
  return mk_rsrc((const void*)(pc + (long long)d1), k2off + K2LEN);
}
__device__ __forceinline__ bf16x8 asb(u32x4 v) { return __builtin_bit_cast(bf16x8, v); }
__device__ __forceinline__ u32x4 asu(bf16x8 v) { return __builtin_bit_cast(u32x4, v); }
__device__ __forceinline__ f32x4 asf(u32x4 v) { return __builtin_bit_cast(f32x4, v); }
__device__ __forceinline__ u32x4 fsu(f32x4 v) { return __builtin_bit_cast(u32x4, v); }
__device__ __forceinline__ float dot8(bf16x8 a, bf16x8 b, float c) {
  c = __builtin_amdgcn_fdot2_f32_bf16(__builtin_shufflevector(a, a, 0, 1), __builtin_shufflevector(b, b, 0, 1), c, false);
  c = __builtin_amdgcn_fdot2_f32_bf16(__builtin_shufflevector(a, a, 2, 3), __builtin_shufflevector(b, b, 2, 3), c, false);
  c = __builtin_amdgcn_fdot2_f32_bf16(__builtin_shufflevector(a, a, 4, 5), __builtin_shufflevector(b, b, 4, 5), c, false);
  c = __builtin_amdgcn_fdot2_f32_bf16(__builtin_shufflevector(a, a, 6, 7), __builtin_shufflevector(b, b, 6, 7), c, false);
  return c;
}
__device__ __forceinline__ unsigned pk2(float a, float b) {
  const f32x2 v = {a, b};
  return __builtin_bit_cast(unsigned, __builtin_convertvector(v, bf16x2));
}
__device__ __forceinline__ u32x4 pk8(f32x4 a, f32x4 b) {
  return (u32x4){pk2(a[0], a[1]), pk2(a[2], a[3]), pk2(b[0], b[1]), pk2(b[2], b[3])};
}
__device__ __forceinline__ float lo_f(unsigned u) { return __builtin_bit_cast(float, u << 16); }
__device__ __forceinline__ float hi_f(unsigned u) { return __builtin_bit_cast(float, u & 0xffff0000u); }
__device__ __forceinline__ bf16x8 ones8() {
  bf16x8 r;
#pragma unroll
  for (int i = 0; i < 8; ++i) r[i] = (__bf16)1.0f;
  return r;
}
__device__ __forceinline__ float xg_add(float v) {
  unsigned x = __builtin_bit_cast(unsigned, v);
  auto r = __builtin_amdgcn_permlane16_swap(x, x, false, false);
  v = __builtin_bit_cast(float, (unsigned)r[0]) + __builtin_bit_cast(float, (unsigned)r[1]);
  x = __builtin_bit_cast(unsigned, v);
  auto r2 = __builtin_amdgcn_permlane32_swap(x, x, false, false);
  return __builtin_bit_cast(float, (unsigned)r2[0]) + __builtin_bit_cast(float, (unsigned)r2[1]);
}
__device__ __forceinline__ float ror8_add(float v) {
  int x = __builtin_bit_cast(int, v);
  int y = __builtin_amdgcn_update_dpp(x, x, 0x128, 0xf, 0xf, false);
  return v + __builtin_bit_cast(float, y);
}
__device__ __forceinline__ unsigned slow_of(const void* p) { return (3u - ((unsigned)(uintptr_t)p >> 7)) & 7u; }
__device__ __forceinline__ int opq(int v) { asm volatile("" : "+v"(v)); return v; }
__device__ __forceinline__ unsigned rdl(unsigned v, int i) { return (unsigned)__builtin_amdgcn_readlane((int)v, i); }
__device__ __forceinline__ int rfl(int v) { return __builtin_amdgcn_readfirstlane(v); }

__device__ __forceinline__ void lds_put(unsigned addr, int val) {
  asm volatile("" : "+s"(addr), "+s"(val)::"memory");
  *(volatile __attribute__((address_space(3))) int*)(uintptr_t)addr = val;
  asm volatile("" ::: "memory");
}
__device__ __forceinline__ int lds_get(unsigned addr) {
  asm volatile("" : "+s"(addr)::"memory");
  const int v = *(volatile __attribute__((address_space(3))) int*)(uintptr_t)addr;
  asm volatile("" ::: "memory");
  return rfl(v);
}
template <int V> struct IC { static constexpr int value = V; };
template <int I, int N, class F>
__device__ __forceinline__ void sfor(F&& f) {
  if constexpr (I < N) { f(IC<I>{}); sfor<I + 1, N>(f); }
}

__device__ __forceinline__ u32x4 gelu8(f32x4 a, f32x4 b, float s) {
  const float c1 = -2.0f * 0.7978845608028654f * 1.4426950408889634f;
  const float c2 = c1 * 0.044715f;
  const float k1 = c1 * s, k2 = c2 * s * s * s, is = __builtin_amdgcn_rcpf(s);
  f32x4 ta = a * a, tb = b * b;
  ta = ta * k2 + k1; tb = tb * k2 + k1;
  ta *= a; tb *= b;
#pragma unroll
  for (int j = 0; j < 4; ++j) { ta[j] = __builtin_amdgcn_exp2f(ta[j]); tb[j] = __builtin_amdgcn_exp2f(tb[j]); }
  ta = ta * is + is; tb = tb * is + is;
#pragma unroll
  for (int j = 0; j < 4; ++j) { ta[j] = __builtin_amdgcn_rcpf(ta[j]); tb[j] = __builtin_amdgcn_rcpf(tb[j]); }
  ta *= a; tb *= b;
  return pk8(ta, tb);
}

__device__ __forceinline__ void gelu_a(f32x4 a, f32x4 b, float s, f32x4& ta, f32x4& tb) {
  const float c1 = -2.0f * 0.7978845608028654f * 1.4426950408889634f;
  const float c2 = c1 * 0.044715f;
  const float k1 = c1 * s, k2 = c2 * s * s * s;
  ta = a * a; tb = b * b;
  ta = ta * k2 + k1; tb = tb * k2 + k1;
  ta *= a; tb *= b;
}
__device__ __forceinline__ void gelu_b(float s, f32x4& ta, f32x4& tb) {
  const float is = __builtin_amdgcn_rcpf(s);
#pragma unroll
  for (int j = 0; j < 4; ++j) { ta[j] = __builtin_amdgcn_exp2f(ta[j]); tb[j] = __builtin_amdgcn_exp2f(tb[j]); }
  ta = ta * is + is; tb = tb * is + is;
}
__device__ __forceinline__ u32x4 gelu_c(f32x4 a, f32x4 b, f32x4 ta, f32x4 tb) {
#pragma unroll
  for (int j = 0; j < 4; ++j) { ta[j] = __builtin_amdgcn_rcpf(ta[j]); tb[j] = __builtin_amdgcn_rcpf(tb[j]); }
  return pk8(ta * a, tb * b);
}

template <bool FIRST>
__device__ __forceinline__ void body(const u16* __restrict__ x0, const u16* __restrict__ wqkv,
                                     const u16* __restrict__ wphi, const u16* __restrict__ wo,
                                     const u16* __restrict__ w1, const u16* __restrict__ w2,
                                     const u16* __restrict__ w3, const u16* __restrict__ w4,
                                     char* __restrict__ ws, u16* __restrict__ out, char* smem) {
  const int b = blockIdx.x;
  const int xcd = b & 7, idx = b >> 3;
  const int cl = (xcd >> 1) * 8 + (idx >> 2), nt = (xcd & 1) * 4 + (idx & 3);
  const int tid = threadIdx.x, w = rfl(tid >> 6), l0 = tid & 63;
  const unsigned lb = lds_addr(smem);
  const u32x4 rs = mk_rsrc(ws, WS_BYTES);
  const int g3c = nt < 3 ? 32 + 2 * nt : 35 + nt;
  const f32x4 zf = {0.f, 0.f, 0.f, 0.f};
  int bud = SPINCAP;
#define SROW(t) (unsigned)(cl * 4 + (t))
#define LANE                                                  \
  const int l = opq(l0), r = l & 15, g = l >> 4;              \
  const unsigned l16 = (unsigned)l * 16;                      \
  (void)r; (void)g; (void)l16;
#define BAR()                                                 \
  asm volatile("s_waitcnt lgkmcnt(0)" ::: "memory");          \
  __builtin_amdgcn_s_barrier();                               \
  asm volatile("" ::: "memory");

  if (w == 8 || w == 9) {
    LANE
    const int kh = w & 1;
    unsigned pf = 0;
    const unsigned prow = (unsigned)((cl & 7) * 64 + l), v512 = prow * 1024 + nt * 128, vff = prow * (FF * 2);
    u32x4 rw_ = mk_rsrc(FIRST ? wqkv : wo, FIRST ? 512 * 1536 * 2 : 512 * 512 * 2);
    VL(unsigned, (kh ? F_LAND + 256 : L_F) + l * 4) = 0u;
    if (kh == 0) VL(unsigned, F_LAND + l * 4) = 0u;
    __builtin_amdgcn_s_setprio(2);
    const unsigned sw = (unsigned)((l & 7) ^ (((l >> 4) & 3) << 1));
    const unsigned rw = (unsigned)(l >> 3) * 2;
    unsigned ld = FIRST ? 1536 : 512;
    unsigned vo = (rw + kh * 512) * ld + (sw << 4) + (FIRST ? 2048 : 0) + nt * 128;
    if constexpr (!FIRST && (PK & 1)) {
      if ((unsigned)nt == slow_of(wo)) {
        rw_ = mk_rsrc(ws + WS_P, 4448 * 128);
        ld = 64;
        vo = (rw + kh * 512) * 64 + (sw << 4) + 3 * 65536;
      }
    }
    const u32x4 rq = mk_rsrc(c_seq, sizeof(c_seq));
    const unsigned lf = lb + F_LAND + kh * 256;
    STD(0)
    if constexpr (FIRST || !(K2B & 4)) { BAR() } else asm volatile("s_waitcnt lgkmcnt(0)" ::: "memory");
    STD(1)
    if constexpr (FIRST) {
      nap<DP0>();
      if constexpr (LIN & 4) { touch(pf, l < 24 ? (unsigned)((b * 48 + kh * 24 + l) * 128) : OOB, rw_); nap<DQ1>(); }
      if constexpr (!(LIN & 2)) touch(pf, prow * 3072 + nt * 128 + (kh ? 1024 : 2048), rw_);
    }
    if constexpr (!FIRST) {
      glds16(wphi + nt * 1024 + kh * 512, l16, lb + L_WPH + kh * 1024);
      unsigned keep;
      asm volatile("s_mov_b32 %0, m0\n\ts_mov_b32 m0, %2\n\ts_nop 0\n\tbuffer_load_dword off, %1, 4 lds\n\ts_mov_b32 m0, %0"
                   : "=&s"(keep) : "s"(rq), "s"(lf) : "memory");
      if constexpr (K2B & 4) { BAR() }
    }
    unsigned tlo = 0, thi = 0, tnb = 0, tld = 0, tc0 = 0;
    int n = 2;
    int prog = 0;
    const int pa = F_PROG + ((l & 3) * 2 + kh) * 4;
    const int nm = FIRST ? 3 : 21;
    int m = FIRST ? 0 : 3;
#pragma unroll 1
    for (int it = 0; it < nm; ++it) {
      sfor<0, 2>([&](auto Q) {
        constexpr int j = decltype(Q)::value;
        const int need = n - 4;
        const unsigned dst = lb + (unsigned)(kh * 4 + (n & 3)) * 16384;
        if (__builtin_expect(__any(prog <= need), 0)) {
          do { prog = (int)VL(unsigned, pa); } while (__any(prog <= need) && --bud > 0);
        }
        prog = (int)VL(unsigned, pa);
        if (n < 21) { STD(n * 3) }
        if ((!FIRST || !SLD) && j && m == 11) {
          if (kh) dma_unit<4>(vo, rw_, j * 256 * ld, ld * 16, dst, rq, (unsigned)n * 4, lf);
          else dma_unit<8>(vo, rw_, j * 256 * ld, ld * 16, dst, rq, (unsigned)n * 4, lf);
        } else if (((FIRST ? THM1 : THM) >> m) & 1) {
          dma_unit<17>(vo, rw_, j * 256 * ld, ld * 16, dst, rq, (unsigned)n * 4, lf);
        } else {
          dma_unit<16>(vo, rw_, j * 256 * ld, ld * 16, dst, rq, (unsigned)n * 4, lf);
        }
        if (n < 21) { STD(n * 3 + 1) }
        if constexpr (!FIRST && j == 0 && !(KT & 4)) {
          if (it == 2) touch(pf, vff + (kh ? g3c * 64 : 1024 + nt * 128), mk_rsrc(w3, 512 * FF * 2));
          if (it == 3) touch(pf, v512 + kh * 512 * 1024, mk_rsrc(w4, 512 * FF * 2));
          if (it == 4 && kh == 0) touch(pf, v512 + 1024 * 1024, mk_rsrc(w4, 512 * FF * 2));
        }
        if constexpr (!FIRST && j == 0 && (OT & 1)) {
          if (it == OTI && kh == 0) touch(pf, (unsigned)(cl * 64 + l) * 1024 + nt * 128, mk_rsrc(out, 2048 * DM * 2));
        }
        if constexpr (FIRST && j == 0) { if (it == 0) glds16(wphi + nt * 1024 + kh * 512, l16, lb + L_WPH + kh * 1024); }
        ++n;
      });
      if constexpr (FIRST && SLD) {
        vo -= 1024;
        if constexpr (PK & 2) { if (it == 1) vo ^= 512; }
        continue;
      }
      if (it == 0) {
        const int t = opq(l0);
        const u16* W = t < 3 ? wqkv : (t == 3 ? wo : (t == 4 ? w1 : (t == 5 ? w2 : (t < 9 ? w3 : w4))));
        tlo = (unsigned)(uintptr_t)W; thi = (unsigned)((uintptr_t)W >> 32) & 0xffffu;
        tld = t < 3 ? 1536 : ((t < 6 || t > 8) ? 512 : FF);
        tnb = t < 3 ? 512 * 1536 * 2 : (t < 6 ? 512 * 512 * 2 : 512 * FF * 2);
        tc0 = t < 2 ? 1024 - 512 * t : (t == 7 ? 512 : (t > 8 ? (t - 9) * 512 * 512 : 0));
        tc0 = (t == 8 ? g3c * 32 : tc0 + nt * 64) * 2;
        if constexpr (PK & 1) {
          if ((t < 6 || t > 8) && (unsigned)nt == slow_of(W)) {
            const char* P = ws + WS_P;
            tlo = (unsigned)(uintptr_t)P; thi = (unsigned)((uintptr_t)P >> 32) & 0xffffu;
            tld = 64; tnb = 4448 * 128;
            tc0 = (t < 6 ? t : t - 3) * 65536;
          }
        }
        tc0 += kh * (t == 11 ? 192 : 256) * tld * 2;
      }
      m = m == 11 ? 0 : m + 1;
      ld = rdl(tld, m);
      rw_ = (u32x4){rdl(tlo, m), rdl(thi, m), rdl(tnb, m), 0x00020000u};
      const unsigned cm = (m == 8 && nt >= 3) ? 3u : 7u;
      vo = rw * ld + ((sw & cm) << 4) + rdl(tc0, m);
    }
    u32x4 cp_;
    unsigned cpo = OOB;
    if constexpr (FIRST && (PK & 1)) {
      const u16* W = kh ? w4 : (g == 0 ? wo : (g == 1 ? w1 : w2));
      unsigned row = kh ? (unsigned)(b * 6 + (l >> 3)) : (unsigned)(b * 2 + ((l >> 3) & 1));
      const bool on = l < 48 && row < FF;
      row = on ? row : 0u;
      const char* src = (const char*)W + row * 1024 + slow_of(W) * 128 + (l & 7) * 16;
      asm volatile("global_load_dwordx4 %0, %1, off" : "=v"(cp_) : "v"(src) : "memory");
      if (on) cpo = ((kh ? 3072u : 1536u + g * 512) + row) * 128 + (l & 7) * 16;
    }
    if constexpr (FIRST) {
      if constexpr (!(KT & 16) || (KT & 32))
        touch(pf, ((PK & 1) && (unsigned)nt == slow_of(kh ? w1 : wo)) ? OOB : v512, mk_rsrc(kh ? w1 : wo, 512 * 1024));
      if constexpr (!(KT & 16)) touch(pf, kh ? vff + nt * 128 : v512, mk_rsrc(kh ? w3 : w2, kh ? 512 * FF * 2 : 512 * 1024));
      if constexpr (KT & 16) touch(pf, l < 16 ? (unsigned)((b * 16 + l) * 128) : OOB, mk_rsrc(kh ? w2 : wo, 512 * 1024));
      if constexpr (OT & 4) { if (kh == 0) touch(pf, l == 0 ? (unsigned)(b * 8192) : OOB, mk_rsrc(out, 2048 * DM * 2)); }
      nap<KTN>();
      if constexpr (KT & 2) touch(pf, vff + (kh ? g3c * 64 : 1024 + nt * 128), mk_rsrc(w3, 512 * FF * 2));
      if constexpr (KT & 1) {
        touch(pf, v512 + kh * 512 * 1024, mk_rsrc(w4, 512 * FF * 2));
        if (kh == 0) touch(pf, v512 + 1024 * 1024, mk_rsrc(w4, 512 * FF * 2));
      }
      if constexpr (KT & 8) {
        touch(pf, l < 24 ? (unsigned)((b * 48 + kh * 24 + l) * 128) : OOB, mk_rsrc(w4, 512 * FF * 2));
      }
    }
    if constexpr (FIRST && PZK > 0) {
      __builtin_amdgcn_s_setprio(0);
      nap<PZ30>();
      const u32x4 pz = {0xffffffffu, 0xffffffffu, 0xffffffffu, 0xffffffffu};
      const unsigned r0 = SROW(kh * 2), r1 = SROW(kh * 2 + 1);
      const unsigned b0 = (r0 * 16 + nt * 2) * 1024, b1 = (r1 * 16 + nt * 2) * 1024;
      const unsigned vs = l < 32 ? (g ? r1 : r0) * 2048 + nt * 256 + r * 16 : OOB;
      auto pz4 = [&](unsigned base) {
        nap<PZ3>(); bstore(pz, l16, rs, base + b0);
        nap<PZ3>(); bstore(pz, l16, rs, base + b0 + 1024);
        nap<PZ3>(); bstore(pz, l16, rs, base + b1);
        nap<PZ3>(); bstore(pz, l16, rs, base + b1 + 1024);
      };
      bstore(pz, vs, rs, WS_SC);
      pz4(WS_B + 2 * MB2);
      if constexpr (PZK > 1) {
        pz4(WS_B + 3 * MB2);
        pz4(WS_B + 4 * MB2);
        const unsigned c0 = WS_C + (r0 * 11 + g3c - 32) * 1024, c1 = WS_C + (r1 * 11 + g3c - 32) * 1024;
        nap<PZ3>(); bstore(pz, l16, rs, c0);
        nap<PZ3>(); bstore(pz, l16, rs, c1);
        if (nt < 3) {
          nap<PZ3>(); bstore(pz, l16, rs, c0 + 1024);
          nap<PZ3>(); bstore(pz, l16, rs, c1 + 1024);
        }
        bstore(pz, vs, rs, WS_SB);
      }
    }
    asm volatile("s_waitcnt vmcnt(0)" : "+v"(pf)::"memory");
    if constexpr (FIRST && (PK & 1)) { tie(cp_); bstore(cp_, cpo, rs, WS_P); }
  } else if (w > 9) {
    const int hw = w - 10;
    int layer = 0;
    (void)layer;
    __builtin_amdgcn_s_setprio(3);
    unsigned pc_ = 0;
    unsigned k2off;
    u32x4 rc;
    if constexpr (FIRST) {
      rc = code_rsrc(k2off);
    }
    if constexpr (FIRST && (LIN & 1)) {
      touch(pc_, l0 < 24 ? (unsigned)((b * 48 + hw * 24 + l0) * 128) : OOB, mk_rsrc(wqkv, 512 * 1536 * 2));
    }
    u32x4 cp_;
    if constexpr (FIRST && (PK & 1))
      pload<0>(cp_, l0 < 24 ? (unsigned)((b * 2 + hw) * 3072 + (l0 >> 3) * 1024 + (l0 & 7) * 16) + slow_of(wqkv) * 128 : OOB,
               mk_rsrc(wqkv, 512 * 1536 * 2));
    ST(8)
    if constexpr (FIRST || !(K2B & 2)) { BAR() }
    ST(9)
    STR(60)
    auto partial = [&](unsigned tg) {
      LANE
      u32x4 oc[4];
#pragma unroll
      for (int c = 0; c < 4; ++c) { const unsigned u = r == c ? 0x3f803f80u : 0u; oc[c] = (u32x4){u, u, u, u}; tie(oc[c]); }
      unsigned vk = (unsigned)(r * 64 + g * 16), vz = r < 4 ? (unsigned)((r >> 1) * 32 * KVREC + 2048 + (r & 1) * 64 + g * 16) : OOB;
      int ov = L_V + hw * 4096 + l * 8, op = L_EX + (hw ^ 1) * 2048 + l * 8;
      asm volatile("" : "+v"(vk), "+v"(vz), "+v"(ov), "+v"(op));
      const unsigned rec = WS_KVP + (unsigned)((nt * 4 + hw * 2) * 32 + cl) * KVREC;
      while (__any(VL(unsigned, F_DEP + (l & 7) * 4) != 1u) && --bud > 0) nap<1>();
      ST(1)
      bf16x8 vb[2][2], pa[4][2];
#pragma unroll
      for (int c = 0; c < 4; ++c) {
        pa[c][0] = tr_frag(smem, op + c * 512, op + c * 512 + 4096);
        if (!(c & 1)) vb[c >> 1][0] = tr_frag(smem, ov + (c >> 1) * 512, ov + (c >> 1) * 512 + 1024);
        pa[c][1] = tr_frag(smem, op + c * 512 + 8192, op + c * 512 + 12288);
        if (!(c & 1)) vb[c >> 1][1] = tr_frag(smem, ov + (c >> 1) * 512 + 2048, ov + (c >> 1) * 512 + 3072);
      }
      SB
      f32x4 kv[4], z = zf;
#pragma unroll
      for (int c = 0; c < 4; ++c) kv[c] = mfma16(pa[c][1], vb[c >> 1][1], mfma16(pa[c][0], vb[c >> 1][0], zf));
      SB
      VL(unsigned, F_PD + hw * 4) = 1u;
#pragma unroll
      for (int c = 0; c < 4; ++c) {
        z = mfma16(pa[c][1], asb(oc[c]), mfma16(pa[c][0], asb(oc[c]), z));
        SB
        bstore(tagged(fsu(kv[c]), tg), vk, rs, rec + (c >> 1) * 32 * KVREC + (c & 1) * 1024);
      }
      bstore(tagged(fsu(z), tg), vz, rs, rec);
      ST(2)
    };
    if constexpr (FIRST) {
      LANE
      ST(0)
      __builtin_amdgcn_s_setprio(0);
      {
        const u32x4 pz = {0xffffffffu, 0xffffffffu, 0xffffffffu, 0xffffffffu};
        nap<PZ0>();
        bstore(pz, (hw == 0 || b < 72) ? l16 : OOB, rs, (hw ? WS_KVR : WS_SA) + (unsigned)b * 1024u);
        const unsigned pb = (hw ? WS_H : WS_B + MB2) + (unsigned)nt * 2048u;
#pragma unroll
        for (int i = 0; i < 8; ++i) {
          nap<PZ1>(); bstore(pz, l16, rs, pb + SROW(i >> 1) * 16384 + (i & 1) * 1024);
        }
      }
      ST(3)
#pragma nounroll
      while (lds_get(lb + F_LAND + hw * 256) < CSL && --bud > 0) nap<2>();
      __builtin_amdgcn_s_setprio(3);
      u32x4 v[8];
      const int u = hw * 128 + (b >> 1), ci = u >> 3, rb = u & 7;
      const int cb = ci < 24 ? (ci >> 2) * 8 + (b & 1) * 4 + (ci & 3) : 24 + (b & 1) * 3 + ci;
      {
        const u16* W; int ld, c0;
        if (cb < 24) { W = wqkv; ld = 1536; c0 = cb * 64; }
        else if (cb < 32) { W = w1; ld = 512; c0 = (cb - 24) * 64; }
        else { W = w3; ld = FF; c0 = (cb - 32) * 64; }
        const int col = c0 + (l & 7) * 8;
        const unsigned vo = (u < 216 && col < ld) ? (unsigned)(((rb * 64 + (l >> 3)) * ld + col) * 2) : OOB;
        const u32x4 rw = mk_rsrc(W, (unsigned)ld * 1024u);
#pragma unroll
        for (int i = 0; i < 8; ++i)
          asm volatile("buffer_load_dwordx4 %0, %1, %2, %3 offen" : "=v"(v[i]) : "v"(vo), "s"(rw), "s"(i * 16 * ld) : "memory");
      }
      SB
      if (hw == 0) touch(pc_, k2off + (unsigned)(l * 128 + (idx % ((K2LEN + 8191) / 8192)) * 8192), rc);
      asm volatile("s_waitcnt vmcnt(0)" : "+v"(pc_)::"memory");
      __builtin_amdgcn_s_setprio(0);
      ST(4)
      {
        const unsigned one = (r & 1) ? 0x3f800000u : 0x00003f80u;
        const u32x4 sel = {(r >> 1) == 0 ? one : 0u, (r >> 1) == 1 ? one : 0u, (r >> 1) == 2 ? one : 0u, (r >> 1) == 3 ? one : 0u};
        f32x4 s = zf;
#pragma unroll
        for (int i = 0; i < 8; ++i) { tie(v[i]); s = mfma16(asb(sel), asb(v[i]), s); }
#pragma unroll
        for (int j = 0; j < 4; ++j) s[j] = ror8_add(s[j]);
        bstore(fsu(s), (u < 216 && r < 8 && g < 2) ? (unsigned)(r * 32 + g * 16) : OOB, rs, WS_CS + (unsigned)(rb * 3456 + cb * 64) * 4);
      }
      SB
      if constexpr (PK & 1) {
        tie(cp_);
        bstore(cp_, l < 24 ? (unsigned)((2 - (l >> 3)) * 65536 + (l & 7) * 16) : OOB, rs, WS_P + (unsigned)(b * 2 + hw) * 128);
      }
      ST(5)
      __builtin_amdgcn_s_setprio(3);
      partial(1u);
      STF
    } else {
      bool fb = true;
      (void)fb;
#pragma unroll 1
      for (layer = 0; layer < 2; ++layer) {
        LANE
        ST(0)
        const unsigned kvr = WS_KVR + (unsigned)layer * 32 * KVR;
        const unsigned kt = (unsigned)layer ^ 1u;
        if (layer) partial(0u);
        u32x4 a[8];
        if (hw == 1) {
          if (layer) nap<DLR>();
          const unsigned sr = WS_KVP + (unsigned)cl * 32 * KVREC;
          const unsigned vr = (unsigned)g * KVREC + (unsigned)(nt * 256 + r * 16);
          for (;;) {
            sfor<0, 8>([&](auto I) { constexpr int i = decltype(I)::value; bload<0>(a[i], vr, rs, sr + i * 4 * KVREC); });
            if constexpr (K2B & 2) { if (fb) { BAR() fb = false; } }
            wait_vm<0>();
            unsigned m = 0;
#pragma unroll
            for (int i = 0; i < 8; ++i) { tie(a[i]); m |= badbits(a[i], kt); }
            if (!__any(m & 1u) || --bud <= 0) break;
            nap<2>();
          }
          ST(3)
          f32x4 s = ((asf(a[0]) + asf(a[1])) + (asf(a[2]) + asf(a[3]))) + ((asf(a[4]) + asf(a[5])) + (asf(a[6]) + asf(a[7])));
#pragma unroll
          for (int j = 0; j < 4; ++j) s[j] = xg_add(s[j]);
          const u32x2 o2 = {pk2(s[0], s[1]) & ~1u, pk2(s[2], s[3])};
          bstore8(o2, g == 0 ? (unsigned)(nt * 128 + r * 8) : OOB, rs, kvr + (unsigned)cl * KVR);
          if constexpr (DKH) nap<DLK>();
          VL(unsigned, F_KV) = (unsigned)(layer + 1);
          ST(4)
        } else if (nt == (cl & 7)) {
          if (layer) nap<DLR>();
          const unsigned sr = WS_KVP + (unsigned)cl * 32 * KVREC + 2048;
          const unsigned vr = (unsigned)(l >> 3) * KVREC + (unsigned)(l & 7) * 16;
          for (;;) {
            sfor<0, 4>([&](auto I) { constexpr int i = decltype(I)::value; bload<0>(a[i], vr, rs, sr + i * 8 * KVREC); });
            if constexpr (K2B & 2) { if (fb) { BAR() fb = false; } }
            wait_vm<0>();
            unsigned m = 0;
#pragma unroll
            for (int i = 0; i < 4; ++i) { tie(a[i]); m |= badbits(a[i], kt); }
            if (!__any(m & 1u) || --bud <= 0) break;
            nap<2>();
          }
          f32x4 s = (asf(a[0]) + asf(a[1])) + (asf(a[2]) + asf(a[3]));
#pragma unroll
          for (int j = 0; j < 4; ++j) s[j] = xg_add(ror8_add(s[j]));
          const u32x2 o2 = {pk2(s[0], s[1]) & ~1u, pk2(s[2], s[3])};
          bstore8(o2, l < 8 ? (unsigned)(1024 + l * 8) : OOB, rs, kvr + (unsigned)cl * KVR);
        }
        if constexpr (K2B & 2) { if (fb) { BAR() fb = false; } }
        ST(5)
        if (layer == 0) {
#pragma nounroll
          while (lds_get(lb + F_KV) == 0 && --bud > 0) nap<1>();
          nap<HN>();
        }
        if (layer == 0) {
          float t[4][8];
#pragma unroll
          for (int uu = 0; uu < 4; ++uu) {
            const int u = hw ? 4 + uu : uu;
            int c0 = 2048 + (u - 4) * 512 + nt * 64 + l;
            if (u == 0) c0 = 512 + nt * 64 + l;
            if (u == 1) c0 = 1024 + nt * 64 + l;
            if (u == 2) c0 = nt * 64 + l;
            if (u == 3) c0 = 1536 + nt * 64 + l;
            if (u >= 6) c0 = 2048 + g3c * 32 + (nt < 3 ? l : (l & 31));
            const float* cp = (const float*)(ws + WS_CS) + c0;
#pragma unroll
            for (int i = 0; i < 8; ++i) t[uu][i] = cp[i * 3456];
          }
#pragma unroll
          for (int uu = 0; uu < 4; ++uu)
            VL(float, L_CS + (hw * 4 + uu) * 256 + l * 4) = ((t[uu][0] + t[uu][1]) + (t[uu][2] + t[uu][3])) + ((t[uu][4] + t[uu][5]) + (t[uu][6] + t[uu][7]));
          VL(unsigned, F_CS + hw * 4) = 1u;
          __builtin_amdgcn_s_setprio(0);
          const u32x4 pz = {0xffffffffu, 0xffffffffu, 0xffffffffu, 0xffffffffu};
          const unsigned r0 = SROW(hw * 2), r1 = SROW(hw * 2 + 1);
          const unsigned b0 = (r0 * 16 + nt * 2) * 1024, b1 = (r1 * 16 + nt * 2) * 1024;
          const unsigned vs = l < 32 ? (g ? r1 : r0) * 2048 + nt * 256 + r * 16 : OOB;
          auto pz4 = [&](unsigned base) {
            nap<PZ2>(); bstore(pz, l16, rs, base + b0);
            nap<PZ2>(); bstore(pz, l16, rs, base + b0 + 1024);
            nap<PZ2>(); bstore(pz, l16, rs, base + b1);
            nap<PZ2>(); bstore(pz, l16, rs, base + b1 + 1024);
          };
          if constexpr (PZK == 0) {
          bstore(pz, vs, rs, WS_SC);
          pz4(WS_B + 2 * MB2);
          wait_vm<0>();
          }
          VL(unsigned, F_PZ + hw * 4) = 1u;
          ST(6)
          if constexpr (PZK < 2) {
          pz4(WS_B + 3 * MB2);
          pz4(WS_B + 4 * MB2);
          const unsigned c0 = WS_C + (r0 * 11 + g3c - 32) * 1024, c1 = WS_C + (r1 * 11 + g3c - 32) * 1024;
          nap<PZ2>(); bstore(pz, l16, rs, c0);
          nap<PZ2>(); bstore(pz, l16, rs, c1);
          if (nt < 3) {
            nap<PZ2>(); bstore(pz, l16, rs, c0 + 1024);
            nap<PZ2>(); bstore(pz, l16, rs, c1 + 1024);
          }
          bstore(pz, vs, rs, WS_SB);
          wait_vm<0>();
          }
          VL(unsigned, F_PZ + hw * 4) = 2u;
          __builtin_amdgcn_s_setprio(3);
          ST(7)
        }
        STF
      }
    }
  } else {
    const int kh = w & 1, rt = w >> 1;
    const unsigned srow = SROW(rt);
    const unsigned sblk = (srow * 16 + nt * 2 + kh) * 1024;
    const unsigned sa = srow * 16384 + kh * 8192;
    bf16x8 B[4][4];
    u32x4 a[8];
    f32x4 acc[4];
    f32x4 res0, res1;
    int o[4];
    int un = 2, landed = 0, xs = 0, xpar = kh;
    float mean = 0.f, rstd = 1.f;
    int layer = 0;
    auto nohook = [](auto) {};
    unsigned lv;
    auto look = [&]() { lv = VL(unsigned, F_LAND + kh * 256); };
    auto need = [&](int n) {
      if (__builtin_expect(landed <= n, 0)) {
#pragma nounroll
        do { landed = lds_get(lb + F_LAND + kh * 256); } while (landed <= n && --bud > 0);
      }
    };
    auto release = [&]() { lds_put(lb + F_PROG + w * 4, un + 1); };
    const unsigned pmid = lb + (rt < 2 ? F_DUM : F_PROG + w * 4);
    auto advance = [&]() {
      ++un;
      const int d = (un & 3) ? 16384 : -3 * 16384;
#pragma unroll
      for (int t = 0; t < 4; ++t) o[t] += d;
    };
    auto rdb = [&](auto I, int t) {
      constexpr int i = decltype(I)::value;
      B[i][t] = tr_frag(smem, o[t] + i * 4096, o[t] + i * 4096 + 2048);
    };
    auto tail = [&]() {
#pragma unroll
      for (int t = 0; t < 4; ++t) rdb(IC<3>{}, t);
      release();
    };
    auto readall = [&]() {
      landed = lds_get(lb + F_LAND + kh * 256);
      need(un);
      sfor<0, 4>([&](auto I) {
#pragma unroll
        for (int t = 0; t < 4; ++t) rdb(I, t);
      });
    };
    auto unit = [&](auto A0_, auto TAIL_, auto pre, auto post) {
      constexpr int A0 = decltype(A0_)::value;
      constexpr int TAIL = decltype(TAIL_)::value & 3;
      constexpr bool LAZY = decltype(TAIL_)::value & 4;
      constexpr bool FRESH = TAIL || (decltype(TAIL_)::value & 8);
      if constexpr (!LAZY) {
        if constexpr (FRESH) landed = rfl((int)lv);
        need(un + 1);
      }
      sfor<0, 4>([&](auto I) {
        constexpr int i = decltype(I)::value;
        pre(IC<A0 + i>{});
        const bf16x8 af = asb(a[A0 + i]);
#pragma unroll
        for (int t = 0; t < 4; ++t) {
          acc[t] = mfma16(B[i][t], af, acc[t]);
          if constexpr (i == 0 && TAIL) rdb(IC<3>{}, t);
          if constexpr (i >= 1 && !LAZY) rdb(IC<(i + 3) & 3>{}, t);
          SB
        }
        if constexpr (i == 0) {
          if constexpr (TAIL == 1) release();
          if constexpr (TAIL == 2) lds_put(pmid, un + 1);
          advance();
        }
        post(IC<A0 + i>{});
        if constexpr (i == LK && !LAZY && !(decltype(TAIL_)::value & 16)) look();
        SB
      });
    };
    auto pass = [&](auto pre, auto post) {
      unit(IC<0>{}, IC<0>{}, pre, post);
      unit(IC<4>{}, IC<1>{}, pre, post);
    };
    auto gpass = [&](auto pre) {
      unit(IC<0>{}, IC<16>{}, pre, nohook);
      unit(IC<4>{}, IC<(GATE == 0 ? 1 : 3) + 4>{}, pre, nohook);
    };
    auto rel_ld = [&]() {
      if constexpr (RA == 0) {
        if constexpr (GATE == 2) lds_put(lb + F_PROG + w * 4, un);
        readall();
        release();
      }
    };
    auto send = [&](f32x4 s0, f32x4 s1, unsigned l16) {
      const int wb = L_EX + rt * 4096 + xpar * 2048;
      VL(f32x4, wb + l16) = s0;
      VL(f32x4, wb + 1024 + l16) = s1;
      ++xs;
      lds_put(lb + F_X + w * 4, xs);
      xpar ^= 1;
    };
    auto recv = [&](f32x4& x0_, f32x4& x1_, unsigned l16, auto mid) {
      const int rb = L_EX + rt * 4096 + xpar * 2048;
      unsigned f = VL(unsigned, F_X + (w ^ 1) * 4);
      x0_ = VL(f32x4, rb + l16);
      x1_ = VL(f32x4, rb + 1024 + l16);
      SB
      mid();
      SB
      if (__builtin_expect(rfl((int)f) < xs, 0)) {
#pragma nounroll
        do {
          f = VL(unsigned, F_X + (w ^ 1) * 4);
          x0_ = VL(f32x4, rb + l16);
          x1_ = VL(f32x4, rb + 1024 + l16);
        } while (rfl((int)f) < xs && --bud > 0);
      }
    };
    f32x4 e0, e1, y0_, y1_;
    unsigned yf;
    auto recv_a = [&](unsigned l16) {
      const int rb = L_EX + rt * 4096 + xpar * 2048;
      yf = VL(unsigned, F_X + (w ^ 1) * 4);
      y0_ = VL(f32x4, rb + l16);
      y1_ = VL(f32x4, rb + 1024 + l16);
    };
    auto recv_b = [&](unsigned l16) {
      if (__builtin_expect(rfl((int)yf) < xs, 0)) {
#pragma nounroll
        do { recv_a(l16); } while (rfl((int)yf) < xs && --bud > 0);
      }
      e0 += y0_; e1 += y1_;
    };
#define ZACC _Pragma("unroll") for (int t = 0; t < 4; ++t) acc[t] = zf;
#define ALOAD(vo, rsrc, so) sfor<0, 8>([&](auto I) { constexpr int i = decltype(I)::value; bload<(i & 3) * 1024>(a[i], vo, rsrc, so); });
#define ACHK(TAG, CNT, N, vo, rsrc, so, EXTRA)                                                                      \
  [&](auto J) {                                                                                                     \
    constexpr int j = decltype(J)::value;                                                                           \
    unsigned tg_;                                                                                                   \
    { constexpr int i = j; (void)i; tg_ = TAG; wait_vm<CNT>(a[j]); }                                                         \
    if (__builtin_expect(bad(a[j], tg_), 0)) {                                                                      \
      wait_vm<0>();                                                                                                 \
      do {                                                                                                          \
        nap<RNAP>();                                                                                                \
        CNTR                                                                                                        \
        sfor<j, N>([&](auto I) {                                                                                    \
          constexpr int i = decltype(I)::value;                                                                     \
          tie(a[i]);                                                                                                \
          if (i == j || bad(a[i], TAG)) breload<(i & 3) * 1024>(a[i], vo, rsrc, so);                                \
        });                                                                                                         \
        EXTRA                                                                                                       \
        wait_vm<0>(a[j]);                                                                                           \
      } while (bad(a[j], tg_) && --bud > 0);                                                                        \
    }                                                                                                               \
  }
#define SRELOAD(TAG, base)                                                                                          \
  tie(s0); tie(s1);                                                                                                 \
  if (bad(s0, TAG)) breload<0>(s0, l16, rs, base + srow * 2048);                                                    \
  if (bad(s1, TAG)) breload<1024>(s1, l16, rs, base + srow * 2048);
    auto put_stats = [&](unsigned TAG, f32x4 v0, f32x4 v1, unsigned base, int r, int g) {
      const f32x4 s4 = v0 + v1, q4 = v0 * v0 + v1 * v1;
      const float s = xg_add((s4[0] + s4[1]) + (s4[2] + s4[3])), q = xg_add((q4[0] + q4[1]) + (q4[2] + q4[3]));
      unsigned su = __builtin_bit_cast(unsigned, s);
      su = (su & ~1u) | TAG;
      bstore8((u32x2){su, __builtin_bit_cast(unsigned, q)}, g == 0 ? (unsigned)(r * 16) : OOB, rs,
              base + srow * 2048 + nt * 256 + kh * 8);
    };
    auto get_stats = [&](unsigned TAG, u32x4& s0, u32x4& s1, unsigned base, unsigned l16) -> float {
      wait_vm<0>(s0); tie(s1);
      if (__builtin_expect(__any((badbits(s0, TAG) | badbits(s1, TAG)) & 1u), 0)) {
        do {
          nap<1>();
          SRELOAD(TAG, base)
          wait_vm<0>(s0); tie(s1);
        } while (__any((badbits(s0, TAG) | badbits(s1, TAG)) & 1u) && --bud > 0);
      }
      const f32x4 t = asf(s0) + asf(s1);
      mean = xg_add(t[0] + t[2]) * (1.0f / DM);
      float var = xg_add(t[1] + t[3]) * (1.0f / DM) - mean * mean;
      var = var < 0.f ? 0.f : var;
      rstd = __builtin_amdgcn_rsqf(var + 1e-5f);
      return var;
    };
    f32x4 n0, n1;
    auto ld_cs = [&](int u, int g) {
      n0 = VL(f32x4, L_CS + u * 256 + kh * 128 + g * 16);
      n1 = VL(f32x4, L_CS + u * 256 + kh * 128 + 64 + g * 16);
    };
    auto app_cs = [&]() { acc[0] -= mean * n0; acc[1] -= mean * n1; };
    auto ini_cs = [&]() { acc[0] = -mean * n0; acc[1] = -mean * n1; acc[2] = zf; acc[3] = zf; };
    auto sub_cs = [&](int u, int g) {
      const f32x4 c0 = VL(f32x4, L_CS + u * 256 + kh * 128 + g * 16), c1 = VL(f32x4, L_CS + u * 256 + kh * 128 + 64 + g * 16);
      acc[0] -= mean * c0;
      acc[1] -= mean * c1;
    };
    auto fm_ops = [&](u32x4 (&wa)[4], int l, int r, int g) {
      const int wo_ = L_WPH + (kh * 2 + ((l >> 1) & 1)) * 512 + (4 * g + (r >> 2)) * 32;
      const unsigned sg = (r & 4) ? 0x80008000u : 0u, m0 = (r & 8) ? 0u : 0xffffffffu;
#pragma unroll
      for (int t = 0; t < 4; ++t) {
        const u32x2 x = __builtin_bit_cast(u32x2, tr4(smem, wo_ + t * 8));
        wa[t] = (u32x4){(x[0] ^ sg) & m0, (x[1] ^ sg) & m0, (x[0] ^ sg) & ~m0, (x[1] ^ sg) & ~m0};
      }
    };
    auto fmap = [&](const u32x4 (&wa)[4], u32x4 kb, u32x4& lo, u32x4& hi) {
      const float L2E = 1.4426950408889634f;
      f32x4 p[4];
#pragma unroll
      for (int t = 0; t < 4; ++t) p[t] = mfma16(asb(wa[t]), asb(kb), zf);
      float m = fmaxf(fmaxf(p[0][0], p[0][1]), fmaxf(p[0][2], p[0][3]));
#pragma unroll
      for (int t = 1; t < 4; ++t) m = fmaxf(fmaxf(fmaxf(m, p[t][0]), p[t][1]), fmaxf(p[t][2], p[t][3]));
      const float ml = -m * L2E;
#pragma unroll
      for (int t = 0; t < 4; ++t)
#pragma unroll
        for (int j = 0; j < 4; ++j) p[t][j] = __builtin_amdgcn_exp2f(__builtin_fmaf(p[t][j], L2E, ml));
      const f32x4 s4 = (p[0] + p[1]) + (p[2] + p[3]);
      const float inv = __builtin_amdgcn_rcpf((s4[0] + s4[1]) + (s4[2] + s4[3]));
      lo = pk8(p[0] * inv, p[1] * inv);
      hi = pk8(p[2] * inv, p[3] * inv);
    };
#define DUMP(v, off)

    float lrr = 0.f, lmr = 0.f;
    auto norm = [&](auto I) {
      constexpr int i = decltype(I)::value;
#pragma unroll
      for (int j = 0; j < 4; ++j)
        a[i][j] = pk2(__builtin_fmaf(lo_f(a[i][j]), lrr, lmr), __builtin_fmaf(hi_f(a[i][j]), lrr, lmr));
    };
    {
      LANE
      ST(0)
      if constexpr (FIRST ? XB == 0 : !(K2B & 1)) { BAR() }
      const int rbase = ((4 * g + (r >> 2)) * 128 + ((2 * (g & 1) + (r >> 3)) << 5) + (l & 3) * 8) ^ (kh << 6);
#pragma unroll
      for (int t = 0; t < 4; ++t) o[t] = (rbase ^ (t << 5)) + kh * 65536 + 32768;
      if constexpr (FIRST) {
        const int xb = kh * 65536 + rt * 8192;
        __builtin_amdgcn_s_setprio(3);
#pragma unroll
        for (int j = 0; j < 8; ++j) {
          const int jj = (j + XROT * (nt & 3)) & 7;
          const int row = 2 * jj + (l >> 5);
          glds16(x0 + (size_t)(srow * 16) * DM + kh * 256, (unsigned)(row * 1024 + (((l & 31) ^ row) << 4)), lb + xb + jj * 1024);
          if (j == 1) nap<XN>();
          if (j + 1 == XB) { BAR() }
        }
        if (rt < 2) __builtin_amdgcn_s_setprio(P1A); else __builtin_amdgcn_s_setprio(P1B);
        int xa[8];
#pragma unroll
        for (int i = 0; i < 8; ++i) xa[i] = opq(xb + r * 512 + (g & 1) * 8 + (((2 * i + (g >> 1)) ^ r) << 4));
        ST(8)
        wait_vm<0>();
        ST(9)
#pragma unroll
        for (int i = 0; i < 8; ++i) {
          const u32x2 lo = VL(u32x2, xa[(2 * i) & 7] + (i >> 2) * 256), hi = VL(u32x2, xa[(2 * i + 1) & 7] + (i >> 2) * 256);
          a[i] = (u32x4){lo[0], lo[1], hi[0], hi[1]};
        }
        lds_put(lb + F_PROG + w * 4, 2);
        f32x4 s1 = zf, s2 = zf;
        const bf16x8 ones = ones8();
#pragma unroll
        for (int i = 0; i < 8; ++i) { s1 = mfma16(ones, asb(a[i]), s1); s2 = mfma16(asb(a[i]), asb(a[i]), s2); }
        const float d01 = (r & 1) ? s2[1] : s2[0], d23 = (r & 1) ? s2[3] : s2[2];
        const float ssum = s1[0], ssq = xg_add(g == (r >> 2) ? ((r & 2) ? d23 : d01) : 0.f);
        const f32x4 sv = {ssum, ssq, 0.f, 0.f};
        send(sv, sv, l16);
        f32x4 x0_, x1_;
        recv(x0_, x1_, l16, [] {});
        const float mu = (ssum + x0_[0]) * (1.0f / DM);
        float var = (ssq + x0_[1]) * (1.0f / DM) - mu * mu;
        var = var < 0.f ? 0.f : var;
        lrr = __builtin_amdgcn_rsqf(var + 1e-5f); lmr = -mu * lrr;
        sfor<0, 8 - LZN>([&](auto I) { norm(I); tie(a[decltype(I)::value]); });
      } else {
        const u32x4 rx = mk_rsrc(x0, 2048 * DM * 2);
        u32x2 lo, hi;
        const unsigned vx = (srow * 16 + r) * (DM * 2) + (nt * 2 + kh) * 64 + g * 8;
        asm volatile("buffer_load_dwordx2 %0, %2, %3, 0 offen\n\tbuffer_load_dwordx2 %1, %2, %3, 0 offen offset:32"
                     : "=&v"(lo), "=&v"(hi) : "v"(vx), "s"(rx) : "memory");
        bload<0>(a[0], l16, rs, WS_B + sblk);
        ST(2)
        if constexpr (K2B & 1) { BAR() }
        ST(3)
        if (rt < 2) __builtin_amdgcn_s_setprio(PRA); else __builtin_amdgcn_s_setprio(PRB);
        asm volatile("s_waitcnt vmcnt(0)" : "+v"(lo), "+v"(hi), "+v"(a[0])::"memory");
        res0 = (f32x4){lo_f(lo[0]), hi_f(lo[0]), lo_f(lo[1]), hi_f(lo[1])};
        res1 = (f32x4){lo_f(hi[0]), hi_f(hi[0]), lo_f(hi[1]), hi_f(hi[1])};
      }
      ST(1)
      if constexpr (FIRST) {
        readall();
        ST(10)
        release();
      }
    }

    auto kv_part = [&](auto pre, auto stats) {
      LANE
      f32x4 x0_, x1_;
      ZACC
      pass(pre, [&](auto I) {
        constexpr int i = decltype(I)::value;
        if constexpr (FIRST && i < LZN) { norm(IC<(8 - LZN + i) & 7>{}); tie(a[(8 - LZN + i) & 7]); }
        if constexpr (!FIRST && i == 6) { stats(); SB ld_cs(1, g); }
        if constexpr (!FIRST && i == 7) { app_cs(); SB ld_cs(0, g); }
      });
      ST(2)
      send(acc[2], acc[3], l16);
      e0 = acc[0]; e1 = acc[1];
      ST(3)
      if constexpr (FIRST) { ZACC } else ini_cs();
      {
        auto post = [&](auto I) {
          constexpr int i = decltype(I)::value;
          if constexpr (i == 0) recv_a(l16);
          if constexpr (i == 1) {
            recv_b(l16);
            const u32x4 vv = pk8(e0 * rstd, e1 * rstd);
            const int pv = L_V + kh * 4096 + rt * 1024 + r * 32 + g * 8;
            VL(u32x2, pv) = (u32x2){vv[0], vv[1]};
            VL(u32x2, pv + 512) = (u32x2){vv[2], vv[3]};
          }
        };
        unit(IC<0>{}, IC<1 + 16>{}, nohook, post);
        unit(IC<4>{}, IC<1 + 4>{}, nohook, nohook);
      }
      ST(4)
      send(acc[2], acc[3], l16);
      {
        u32x4 wa[4], plo, phi;
        recv(x0_, x1_, l16, [&] {
          fm_ops(wa, l, r, g);
        });
        fmap(wa, pk8((acc[0] + x0_) * rstd, (acc[1] + x1_) * rstd), plo, phi);
        const int pp = L_EX + rt * 4096 + xpar * 2048 + g * 512 + r * 32;
        VL(u32x4, pp) = plo;
        VL(u32x4, pp + 16) = phi;
        lds_put(lb + F_DEP + w * 4, 1);
      }
      if constexpr (QP & (FIRST ? 1 : 2)) __builtin_amdgcn_s_setprio(0);
      ST(5)
      readall();
      ZACC
      gpass(nohook);
      ST(6)
      if constexpr (FIRST && QX) {
#pragma nounroll
        while (__any(VL(int, F_PROG + ((l & 3) * 2 + kh) * 4) < 5) && --bud > 0) {}
        const int wb = kh * 65536 + rt * 2048 + l16;
        VL(f32x4, wb) = acc[2];
        VL(f32x4, wb + 1024) = acc[3];
        ++xs;
        lds_put(lb + F_X + w * 4, xs);
        unsigned f;
#pragma nounroll
        do {
          f = VL(unsigned, F_X + (w ^ 1) * 4);
          x0_ = VL(f32x4, wb ^ 65536);
          x1_ = VL(f32x4, (wb ^ 65536) + 1024);
        } while (rfl((int)f) < xs && --bud > 0);
      } else {
#pragma nounroll
        while (lds_get(lb + F_PD + kh * 4) == 0 && --bud > 0) {}
        send(acc[2], acc[3], l16);
        recv(x0_, x1_, l16, [&] {
          if constexpr (!FIRST) sub_cs(2, g);
        });
      }
      a[0] = pk8((acc[0] + x0_) * rstd, (acc[1] + x1_) * rstd);
    };

    if constexpr (FIRST) {
      LANE
      kv_part(nohook, [] {});
      bstore(tagged(a[0], 1u), l16, rs, WS_B + (sblk ^ ((PK & 2) ? 8192u : 0u)));
      ST(7)
      STF
    } else {
#pragma unroll 1
      for (layer = 0; layer < 2; ++layer) {
        const unsigned lt = (unsigned)layer, bl = WS_B + lt * MB2;
        if (layer) {
          LANE
          ST(0)
          u32x4 s0, s1;
          nap<DLX>();
          const unsigned so = WS_B + sa;
          ALOAD(l16, rs, so + (i >> 2) * 4096)
          bload<0>(s0, l16, rs, WS_SB + srow * 2048);
          bload<1024>(s1, l16, rs, WS_SB + srow * 2048);
          readall();
          release();
          ST(1)
          kv_part(ACHK(1, 9 - i, 8, l16, rs, so + (i >> 2) * 4096, SRELOAD(0, WS_SB)),
                  [&] { get_stats(0u, s0, s1, WS_SB, l16); });
        }
        {
          LANE
          u32x4 q[2][2];
          STC(31)
          {
            u32x4 wa[4], plo, phi;
            need(1);
            fm_ops(wa, l, r, g);
            fmap(wa, a[0], plo, phi);
            const unsigned m0 = g < 2 ? 0xffffffffu : 0u;
            q[0][0] = plo & m0; q[0][1] = phi & m0; q[1][0] = plo & ~m0; q[1][1] = phi & ~m0;
            if constexpr (PIN & 2) { tie(q[0][0]); tie(q[0][1]); tie(q[1][0]); tie(q[1][1]); }
          }
          if constexpr (QP & 2) __builtin_amdgcn_s_setprio(PRA);
          ST(7)
#pragma nounroll
          while (lds_get(lb + F_KV) != layer + 1 && --bud > 0) nap<1>();
          if constexpr (!DKH) nap<DLK>();
          u32x4 k0, k1, z0, z1;
          {
            const unsigned kr = WS_KVR + (lt * 32 + nt * 4 + kh * 2) * KVR;
            const unsigned vk = (unsigned)((g >> 1) * KVR + (g & 1) * 512 + r * 32), vz = (unsigned)((g >> 1) * KVR + 1024 + (g & 1) * 32);
            for (;;) {
              bload<0>(k0, vk, rs, kr);
              bload<16>(k1, vk, rs, kr);
              bload<0>(z0, vz, rs, kr);
              bload<16>(z1, vz, rs, kr);
              wait_vm<0>();
              tie(k0); tie(k1); tie(z0); tie(z1);
              if (!__any((badbits(k0, 0u) | badbits(k1, 0u) | badbits(z0, 0u) | badbits(z1, 0u)) & 1u) || --bud <= 0) break;
              nap<KN>();
            }
          }
          ST(8)
          f32x4 on[2];
#pragma unroll
          for (int h = 0; h < 2; ++h) {
            const f32x4 dn = mfma16(asb(z1), asb(q[h][1]), mfma16(asb(z0), asb(q[h][0]), zf));
            const f32x4 nu = mfma16(asb(k1), asb(q[h][1]), mfma16(asb(k0), asb(q[h][0]), zf));
            on[h] = nu * __builtin_amdgcn_rcpf(dn[0] + 1e-6f);
          }
          const u32x4 ov = tagged(pk8(on[0], on[1]), lt);
          bstore(ov, l16, rs, bl + sblk);
          lds_put(lb + F_PROG + w * 4, un - (GATE ? OREL : 0));
          DUMP(ov, (2u << 20) + sblk)
        }
        ST(9)
        {
          LANE
          f32x4 x0_, x1_;
          NAPL(D0A, DLY)
          const unsigned so = bl + sa;
          ALOAD(l16, rs, so + (i >> 2) * 4096)
          rel_ld();
          acc[0] = res0; acc[1] = res1; acc[2] = zf; acc[3] = zf;
          gpass(ACHK(lt, 7 - i, 8, l16, rs, so + (i >> 2) * 4096, ));
          ST(10)
          STC(26)
          send(acc[2], acc[3], l16);
          recv(x0_, x1_, l16, [] {});
          res0 = acc[0] + x0_; res1 = acc[1] + x1_;
          const u32x4 ov = tagged(pk8(res0, res1), lt);
          bstore(ov, l16, rs, bl + MB2 + sblk);
          DUMP(ov, (4u << 20) + sblk)
          put_stats(lt, res0, res1, WS_SA, r, g);
        }
        ST(11)
        {
          LANE
          f32x4 x0_, x1_;
          u32x4 s0, s1;
          float scale;
          NAPL(D0B, DLY - 1)
          const unsigned so = bl + MB2 + sa;
          ALOAD(l16, rs, so + (i >> 2) * 4096)
          bload<0>(s0, l16, rs, WS_SA + srow * 2048);
          bload<1024>(s1, l16, rs, WS_SA + srow * 2048);
          rel_ld();
          ZACC
          gpass(ACHK(lt, 9 - i, 8, l16, rs, so + (i >> 2) * 4096, SRELOAD(lt, WS_SA)));
          ST(12)
          STC(27)
          send(acc[2], acc[3], l16);
          if (layer == 0) {
#pragma nounroll
            while ((lds_get(lb + F_PZ) == 0 || lds_get(lb + F_PZ + 4) == 0) && --bud > 0) {}
          }
          recv(x0_, x1_, l16, [&] {
            const float var = get_stats(lt, s0, s1, WS_SA, l16);
            scale = rstd * __builtin_amdgcn_rsqf(var * rstd * rstd + 1e-5f);
            sub_cs(3, g);
          });
          const u32x4 hv = tagged(gelu8(acc[0] + x0_, acc[1] + x1_, scale), lt);
          bstore(hv, l16, rs, WS_H + sblk);
          DUMP(hv, (6u << 20) + sblk)
        }
        ST(13)
        {
          LANE
          f32x4 x0_, x1_;
          const u32x4 rh = mk_rsrc(ws + WS_H, 2u << 20);
          const int s0 = (int)srow * 16 + r - (1 + kh), s1 = (int)srow * 16 + r + (1 + kh);
          const unsigned v0 = (unsigned)((s0 >> 4) * 16384 + kh * 8192 + ((s0 & 15) + 16 * g) * 16);
          const unsigned v1 = (unsigned)((s1 >> 4) * 16384 + kh * 8192 + 4096 + ((s1 & 15) + 16 * g) * 16);
          const unsigned z0 = 0u;
          const unsigned t0 = s0 < 0 ? 0u : (unsigned)layer, t1 = s1 >= 2048 ? 0u : (unsigned)layer;
          NAPL(D0C, DLY)
          ALOAD((i < 4 ? v0 : v1), rh, z0)
          rel_ld();
          acc[0] = (res0 - mean) * rstd; acc[1] = (res1 - mean) * rstd; acc[2] = zf; acc[3] = zf;
          if constexpr (PIN & 4) asm volatile("" : "+v"(acc[0]), "+v"(acc[1]));
          gpass(ACHK((i < 4 ? t0 : t1), 7 - i, 8, (i < 4 ? v0 : v1), rh, z0, ));
          ST(14)
          STC(28)
          send(acc[2], acc[3], l16);
          if (layer == 0) {
#pragma nounroll
            while (lds_get(lb + F_PZ + (rt >> 1) * 4) != 2 && --bud > 0) {}
          }
          recv(x0_, x1_, l16, [] {});
          const f32x4 y0 = acc[0] + x0_, y1 = acc[1] + x1_;
          const u32x4 ov = tagged(pk8(y0, y1), lt);
          bstore(ov, l16, rs, bl + 2 * MB2 + sblk);
          DUMP(ov, (8u << 20) + sblk)
          put_stats(lt, y0, y1, WS_SC, r, g);
          lds_put(lb + F_PROG + w * 4, un);
          res0 += y0; res1 += y1;
          asm volatile("" : "+v"(res0), "+v"(res1));
        }
        ST(15)
        {
          LANE
          f32x4 x0_, x1_;
          u32x4 s0, s1;
          NAPL(D0D, DLY - 1)
          const unsigned so = bl + 2 * MB2 + sa;
          ALOAD(l16, rs, so + (i >> 2) * 4096)
          bload<0>(s0, l16, rs, WS_SC + srow * 2048);
          bload<1024>(s1, l16, rs, WS_SC + srow * 2048);
          rel_ld();
          ZACC
          pass(ACHK(lt, 9 - i, 8, l16, rs, so + (i >> 2) * 4096, SRELOAD(lt, WS_SC)),
               [&](auto I) {
                 constexpr int i = decltype(I)::value;
                 if constexpr (i == 6) { get_stats(lt, s0, s1, WS_SC, l16); SB ld_cs(4, g); }
                 if constexpr (i == 7) { app_cs(); SB ld_cs(5, g); }
               });
          ST(16)
          STC(29)
          send(acc[2], acc[3], l16);
          e0 = acc[0]; e1 = acc[1];
          ST(17)
          const unsigned bb = layer ? WS_B : WS_B + 4 * MB2;
          const unsigned sha = bl + 3 * MB2 + sa, shb = bb + sa;
          const unsigned cb = WS_C + srow * 11 * 1024;
          ini_cs();
          {
            auto post = [&](auto I) {
              constexpr int i = decltype(I)::value;
              if constexpr (i == 0) { SF(26) recv_a(l16); }
              if constexpr (i == 7) ld_cs(6, g);
              if constexpr (i == 1) { SF(27) recv_b(l16); SF(28) gelu_a(e0, e1, rstd, y0_, y1_); }
              if constexpr (i == 2) gelu_b(rstd, y0_, y1_);
              if constexpr (i == 3) { SF(29) }
              if constexpr (i == 4) { SF(30) }
              if constexpr (i == 6) { SF(31) }
              if constexpr (i == 3) {
                const u32x4 hv = tagged(gelu_c(e0, e1, y0_, y1_), lt);
                bstore(hv, l16, rs, bl + 3 * MB2 + sblk);
                DUMP(hv, (12u << 20) + (srow * 43 + nt * 2 + kh) * 1024)
              }
            };
            unit(IC<0>{}, IC<1>{}, nohook, post);
            unit(IC<4>{}, IC<1>{}, nohook, post);
          }
          ST(18)
          send(acc[2], acc[3], l16);
          e0 = acc[0]; e1 = acc[1];
          ST(19)
          ini_cs();
          {
            auto post = [&](auto I) {
              constexpr int i = decltype(I)::value;
              if constexpr (i == 0) recv_a(l16);
              if constexpr (i == 1) { recv_b(l16); gelu_a(e0, e1, rstd, y0_, y1_); }
              if constexpr (i == 2) gelu_b(rstd, y0_, y1_);
              if constexpr (i == 3) {
                const u32x4 hv = tagged(gelu_c(e0, e1, y0_, y1_), 0u);
                bstore(hv, l16, rs, bb + sblk);
                DUMP(hv, (12u << 20) + (srow * 43 + 16 + nt * 2 + kh) * 1024)
              }
              if constexpr (i >= HS) {
                constexpr int j0 = i == HS ? 0 : (i - HS) * 8 / (8 - HS), j1 = (i + 1 - HS) * 8 / (8 - HS);
                sfor<j0, j1>([&](auto J) { constexpr int j = decltype(J)::value; bload<(j & 3) * 1024>(a[j], l16, rs, sha + (j >> 2) * 4096); });
              }
            };
            unit(IC<0>{}, IC<1>{}, nohook, post);
            unit(IC<4>{}, IC<1 + 16>{}, nohook, post);
          }
          ST(20)
          send(acc[2], acc[3], l16);
          recv(x0_, x1_, l16, tail);
          look();
          {
            const u32x4 hv = tagged(gelu8(acc[0] + x0_, acc[1] + x1_, rstd), lt);
            const bool own = kh == 0 || nt < 3;
            bstore(hv, own ? l16 : OOB, rs, cb + (unsigned)(g3c - 32 + kh) * 1024);
          }
          ST(21)
          acc[0] = res0; acc[1] = res1; acc[2] = zf; acc[3] = zf;
          {
            auto pre = ACHK(lt, 8, 8, l16, rs, sha + (i >> 2) * 4096, );
            auto post = [&](auto I) { constexpr int i = decltype(I)::value; bload<(i & 3) * 1024>(a[i], l16, rs, shb + (i >> 2) * 4096); };
            unit(IC<0>{}, IC<8>{}, pre, post);
            unit(IC<4>{}, IC<1>{}, pre, post);
          }
          ST(22)
          const unsigned sc = cb + kh * 6 * 1024, sc5 = cb + (4 + kh * 5) * 1024;
          {
            auto pre = ACHK(0u, (i < 7 ? 7 : 6), 8, l16, rs, shb + (i >> 2) * 4096, );
            auto post = [&](auto I) {
              constexpr int i = decltype(I)::value;
              if constexpr (i < 5) bload<(i & 3) * 1024>(a[i], l16, rs, sc + (i >> 2) * 4096);
              if constexpr (i == 5) bload<1024>(a[5], l16, rs, sc5);
            };
            unit(IC<0>{}, IC<1>{}, pre, post);
            unit(IC<4>{}, IC<1>{}, pre, post);
          }
          ST(23)
          {
            auto pre = ACHK(lt, 5 - i, 6, l16, rs, (i < 5 ? sc + (i >> 2) * 4096 : sc5), );
            unit(IC<0>{}, IC<2 + 16>{}, pre, nohook);
            pre(IC<4>{});
#pragma unroll
            for (int t = 0; t < 4; ++t) acc[t] = mfma16(B[0][t], asb(a[4]), acc[t]);
            pre(IC<5>{});
            if (kh == 0) {
#pragma unroll
              for (int t = 0; t < 4; ++t) acc[t] = mfma16(B[1][t], asb(a[5]), acc[t]);
            }
          }
          ST(24)
          STC(30)
          send(acc[2], acc[3], l16);
          recv(x0_, x1_, l16, [] {});
          res0 = acc[0] + x0_; res1 = acc[1] + x1_;
          const u32x4 ov = pk8(res0, res1);
          if (layer == 0) {
            bstore(tagged(ov, 1u), l16, rs, WS_B + sblk);
            DUMP(ov, (10u << 20) + sblk)
            put_stats(0u, res0, res1, WS_SB, r, g);
            release();
            advance();
          } else {
            const u32x4 ro = mk_rsrc(out, 2048 * DM * 2);
            const unsigned vo = (unsigned)(r * (DM * 2) + g * 8), so_ = srow * 16 * (DM * 2) + (nt * 2 + kh) * 64;
            const u32x2 lo = {ov[0], ov[1]}, hi = {ov[2], ov[3]};
            asm volatile("s_nop 4\n\tbuffer_store_dwordx2 %0, %2, %3, %4 offen\n\tbuffer_store_dwordx2 %1, %2, %3, %4 offen offset:32\n\ts_nop 1"
                         ::"v"(lo), "v"(hi), "v"(vo), "s"(ro), "s"(so_) : "memory");
          }
        }
        ST(25)
        STF
      }
    }
  }
}

extern "C" __global__ __launch_bounds__(768) NOPK1 void hk1(const u16* __restrict__ x0, const u16* __restrict__ wqkv,
                                          const u16* __restrict__ wphi, const u16* __restrict__ wo,
                                          const u16* __restrict__ w1, const u16* __restrict__ w2,
                                          const u16* __restrict__ w3, const u16* __restrict__ w4,
                                          char* __restrict__ ws, u16* __restrict__ out) {
  __shared__ __attribute__((aligned(1024))) char smem[L_END];
  body<true>(x0, wqkv, wphi, wo, w1, w2, w3, w4, ws, out, smem);
}
extern "C" __global__ __launch_bounds__(768) NOPK2 void hk2(const u16* __restrict__ x0, const u16* __restrict__ wqkv,
                                          const u16* __restrict__ wphi, const u16* __restrict__ wo,
                                          const u16* __restrict__ w1, const u16* __restrict__ w2,
                                          const u16* __restrict__ w3, const u16* __restrict__ w4,
                                          char* __restrict__ ws, u16* __restrict__ out) {
  __shared__ __attribute__((aligned(1024))) char smem[L_END];
  body<false>(x0, wqkv, wphi, wo, w1, w2, w3, w4, ws, out, smem);
}

extern "C" void kernel_launch(void* const* d_in, const int* in_sizes, int n_in, void* d_out, int out_size,
                              void* d_ws, size_t ws_size, hipStream_t stream) {
  const u16* x0 = (const u16*)d_in[0];
  const u16* wqkv = (const u16*)d_in[1];
  const u16* wphi = (const u16*)d_in[2];
  const u16* wo = (const u16*)d_in[3];
  const u16* w1 = (const u16*)d_in[5];
  const u16* w2 = (const u16*)d_in[6];
  const u16* w3 = (const u16*)d_in[7];
  const u16* w4 = (const u16*)d_in[8];
  char* ws = (char*)d_ws;
  hk1<<<256, 768, 0, stream>>>(x0, wqkv, wphi, wo, w1, w2, w3, w4, ws, (u16*)d_out);
  hk2<<<256, 768, 0, stream>>>(x0, wqkv, wphi, wo, w1, w2, w3, w4, ws, (u16*)d_out);
}
```
